```python
import torch

def _softmax(x, axis=-1):
    return torch.softmax(x, dim=axis)
def _gelu(x):
    return torch.nn.functional.gelu(x, approximate="tanh")
def _silu(x):
    return torch.nn.functional.silu(x)
def _sigmoid(x):
    return torch.sigmoid(x)
def _layernorm(x, axis=-1, eps=1e-5):
    m = x.mean(dim=axis, keepdim=True)
    v = x.var(dim=axis, keepdim=True, unbiased=False)
    return (x - m) / torch.sqrt(v + eps)
def _scatter_add(tgt, idx, src, axis=0):
    return tgt.clone().index_add_(axis, idx, src)
def _segment_sum(src, seg, n):
    out = torch.zeros((n,) + src.shape[1:], dtype=src.dtype)
    return out.index_add_(0, seg, src)
def _pad(x, pw):
    flat = tuple(v for pair in reversed(pw) for v in pair)
    return torch.nn.functional.pad(x, flat)

kernel_name = 'standalone__detection_seg_dino__vision'

def setup_inputs():

    _w = (lambda a: (lambda t: t.to(torch.bfloat16) if t.is_floating_point() else t)(torch.as_tensor(a)))
    H = W = (384 // 2) * 2
    C = 96
    Nq = 16
    sc = C ** -0.5
    return {
        "q": _w((lambda *s: torch.randn(*s, dtype=torch.bfloat16) * 0.05)(1, Nq, C)),
        "f0": _w((lambda *s: torch.randn(*s, dtype=torch.bfloat16) * 0.8)(1, H, W, C)),
        "f1": _w((lambda *s: torch.randn(*s, dtype=torch.bfloat16) * 0.8)(1, H // 2, W // 2, C)),
        "wq": _w((lambda *s: torch.randn(*s, dtype=torch.bfloat16) * 3.2)(C, C) * sc),
        "wk": _w((lambda *s: torch.randn(*s, dtype=torch.bfloat16) * 3.2)(C, C) * sc),
        "wv": _w((lambda *s: torch.randn(*s, dtype=torch.bfloat16) * 0.8)(C, C) * sc),
        "wo": _w((lambda *s: torch.randn(*s, dtype=torch.bfloat16) * 0.8)(C, C) * sc),
    }

def reference(q, f0, f1, wq, wk, wv, wo):
    B, H, W, C = f0.shape
    f1u = (lambda x, n, axis: torch.repeat_interleave(x, n, dim=axis))((lambda x, n, axis: torch.repeat_interleave(x, n, dim=axis))(f1, 2, axis=1), 2, axis=2)
    fused = f0 + f1u
    feats = _layernorm(fused.reshape(B, H * W, C), axis=-1)
    qn = _layernorm(q, axis=-1)
    Q = torch.einsum("bnc,cd->bnd", qn, wq)
    K = torch.einsum("bmc,cd->bmd", feats, wk)
    V = torch.einsum("bmc,cd->bmd", feats, wv)
    scale = C ** -0.5
    logits = torch.einsum("bnd,bmd->bnm", Q, K) * scale
    attn = _softmax(logits, axis=-1)
    ctx = torch.einsum("bnm,bmd->bnd", attn, V)
    return q + torch.einsum("bnd,dc->bnc", ctx, wo)
```

```cpp
#include <hip/hip_runtime.h>
#include <hip/hip_bf16.h>
typedef unsigned short u16;
__device__ __forceinline__ float bf2f(u16 x){ return __uint_as_float(((unsigned)x)<<16); }
__device__ __forceinline__ u16 f2bf(float f){ __hip_bfloat16 h = __float2bfloat16(f); return *(u16*)&h; }
constexpr int C=96, NQ=16, H=384, W=384, NP=H*W;
constexpr int NB=576, PB=NP/NB;
__global__ void k_prep(const u16* q, const u16* wq, const u16* wk, float* qk){
  __shared__ float qn[NQ][C], Q[NQ][C];
  int t=threadIdx.x;
  if(t<NQ){ float m=0; for(int c=0;c<C;c++) m+=bf2f(q[t*C+c]); m/=C; float v=0; for(int c=0;c<C;c++){float d=bf2f(q[t*C+c])-m; v+=d*d;} v/=C; float r=rsqrtf(v+1e-5f);
    for(int c=0;c<C;c++) qn[t][c]=(bf2f(q[t*C+c])-m)*r; }
  __syncthreads();
  for(int i=t;i<NQ*C;i+=blockDim.x){int n=i/C,d=i%C; float s=0; for(int c=0;c<C;c++) s+=qn[n][c]*bf2f(wq[c*C+d]); Q[n][d]=s;}
  __syncthreads();
  for(int i=t;i<NQ*C;i+=blockDim.x){int n=i/C,c=i%C; float s=0; for(int d=0;d<C;d++) s+=Q[n][d]*bf2f(wk[c*C+d]); qk[i]=s*0.10206207261596577f;}
}
__global__ void __launch_bounds__(256) k_main(const u16* f0,const u16* f1,const float* qk,float* pm,float* pl,float* pS){
  __shared__ float X[PB][C+1];
  __shared__ float P[NQ][PB];
  __shared__ float sqk[NQ][C];
  __shared__ float sm[NQ];
  int t=threadIdx.x; int b=blockIdx.x;
  for(int i=t;i<NQ*C;i+=256) sqk[i/C][i%C]=qk[i];
  int p=b*PB+t; int y=p/W,x=p%W; const u16* a=f0+(size_t)p*C; const u16* bb=f1+((size_t)(y/2)*(W/2)+x/2)*C;
  float m=0; for(int c=0;c<C;c++){ float v=bf2f(f2bf(bf2f(a[c])+bf2f(bb[c]))); X[t][c]=v; m+=v;} m/=C;
  float v=0; for(int c=0;c<C;c++){float d=X[t][c]-m; v+=d*d;} v/=C; float r=rsqrtf(v+1e-5f);
  for(int c=0;c<C;c++) X[t][c]=(X[t][c]-m)*r;
  __syncthreads();
  for(int n=0;n<NQ;n++){ float s=0; for(int c=0;c<C;c++) s+=sqk[n][c]*X[t][c]; P[n][t]=s; }
  __syncthreads();
  if(t<NQ){ float mx=-1e30f; for(int i=0;i<PB;i++) mx=fmaxf(mx,P[t][i]); sm[t]=mx; float l=0; for(int i=0;i<PB;i++){ float e=__expf(P[t][i]-mx); P[t][i]=e; l+=e;} pm[b*NQ+t]=mx; pl[b*NQ+t]=l; }
  __syncthreads();
  for(int i=t;i<NQ*C;i+=256){int n=i/C,c=i%C; float s=0; for(int j=0;j<PB;j++) s+=P[n][j]*X[j][c]; pS[(size_t)b*NQ*C+i]=s;}
}
__global__ void k_fin(const u16* q,const u16* wv,const u16* wo,const float* pm,const float* pl,const float* pS,u16* out){
  __shared__ float S[C],V[C]; int n=blockIdx.x,t=threadIdx.x;
  float M=-1e30f; for(int b=0;b<NB;b++) M=fmaxf(M,pm[b*NQ+n]);
  float L=0,s=0; for(int b=0;b<NB;b++){ float w=__expf(pm[b*NQ+n]-M); L+=w*pl[b*NQ+n]; if(t<C) s+=w*pS[(size_t)b*NQ*C+n*C+t]; }
  if(t<C) S[t]=s/L; __syncthreads();
  if(t<C){ float a=0; for(int c=0;c<C;c++) a+=S[c]*bf2f(wv[c*C+t]); V[t]=a;} __syncthreads();
  if(t<C){ float a=0; for(int d=0;d<C;d++) a+=V[d]*bf2f(wo[d*C+t]); out[n*C+t]=f2bf(bf2f(q[n*C+t])+a);}
}
extern "C" void kernel_launch(void* const* d_in, const int* in_sizes, int n_in, void* d_out, int out_size, void* d_ws, size_t ws_size, hipStream_t stream){
  const u16* q=(const u16*)d_in[0]; const u16* f0=(const u16*)d_in[1]; const u16* f1=(const u16*)d_in[2];
  const u16* wq=(const u16*)d_in[3]; const u16* wk=(const u16*)d_in[4]; const u16* wv=(const u16*)d_in[5]; const u16* wo=(const u16*)d_in[6];
  float* ws=(float*)d_ws; float* qk=ws; float* pm=ws+2048; float* pl=pm+NB*NQ; float* pS=pl+NB*NQ;
  k_prep<<<1,256,0,stream>>>(q,wq,wk,qk);
  k_main<<<NB,256,0,stream>>>(f0,f1,qk,pm,pl,pS);
  k_fin<<<NQ,128,0,stream>>>(q,wv,wo,pm,pl,pS,(u16*)d_out);
}
```

```cpp
#include <hip/hip_runtime.h>
#include <hip/hip_bf16.h>
#pragma clang diagnostic ignored "-Wint-to-pointer-cast"

typedef unsigned short u16;
typedef __attribute__((ext_vector_type(4))) short s16x4;
typedef __attribute__((ext_vector_type(8))) __bf16 bf16x8;
typedef __attribute__((ext_vector_type(2))) __bf16 bf16x2;
typedef __attribute__((ext_vector_type(2))) float f32x2;
typedef __attribute__((ext_vector_type(4))) float f32x4;
typedef __attribute__((ext_vector_type(4))) unsigned u32x4;
typedef __attribute__((ext_vector_type(3))) unsigned u32x3;
typedef __attribute__((ext_vector_type(2))) unsigned u32x2;
#define LAS __attribute__((address_space(3)))
#define DEVI __device__ __forceinline__

constexpr int NWG = 256;
constexpr int NSW = 9;
constexpr int REG = 15360;
constexpr int L_WK = NSW * REG;
constexpr int L_QK = L_WK + 18432;
constexpr int L_SQ = L_QK + 3072;
constexpr int L_ML = L_SQ + 64;
constexpr int L_END = L_ML + NSW * 128;
constexpr float EPS = 1e-5f;
constexpr float LOG2E = 1.4426950408889634f;

DEVI float bflo(unsigned x) { return __uint_as_float(x << 16); }
DEVI float bfhi(unsigned x) { return __uint_as_float(x & 0xffff0000u); }
DEVI unsigned pk(float a, float b) { f32x2 p = {a, b}; bf16x2 h = __builtin_convertvector(p, bf16x2); return __builtin_bit_cast(unsigned, h); }
DEVI float dot2(unsigned a, unsigned b, float c) { return __builtin_amdgcn_fdot2_f32_bf16(__builtin_bit_cast(bf16x2, a), __builtin_bit_cast(bf16x2, b), c, false); }
DEVI f32x4 mma32(u32x4 a, u32x4 b, f32x4 c) { return __builtin_amdgcn_mfma_f32_16x16x32_bf16(__builtin_bit_cast(bf16x8, a), __builtin_bit_cast(bf16x8, b), c, 0, 0, 0); }
DEVI f32x4 mma16(u32x2 a, u32x2 b, f32x4 c) { return __builtin_amdgcn_mfma_f32_16x16x16bf16_1k(__builtin_bit_cast(s16x4, a), __builtin_bit_cast(s16x4, b), c, 0, 0, 0); }
DEVI float xg_sum(float x) {
  auto s = __builtin_amdgcn_permlane16_swap(__float_as_uint(x), __float_as_uint(x), false, false);
  x = __uint_as_float(s[0]) + __uint_as_float(s[1]);
  auto t = __builtin_amdgcn_permlane32_swap(__float_as_uint(x), __float_as_uint(x), false, false);
  return __uint_as_float(t[0]) + __uint_as_float(t[1]);
}
DEVI float xg_max(float x) {
  auto s = __builtin_amdgcn_permlane16_swap(__float_as_uint(x), __float_as_uint(x), false, false);
  x = fmaxf(__uint_as_float(s[0]), __uint_as_float(s[1]));
  auto t = __builtin_amdgcn_permlane32_swap(__float_as_uint(x), __float_as_uint(x), false, false);
  return fmaxf(__uint_as_float(t[0]), __uint_as_float(t[1]));
}
DEVI u32x4 lds128(unsigned a) { return *(const LAS u32x4*)a; }
DEVI u32x2 ldstr(unsigned a) { s16x4 t = __builtin_amdgcn_ds_read_tr16_b64_v4i16((LAS s16x4*)a); return __builtin_bit_cast(u32x2, t); }
DEVI __amdgpu_buffer_rsrc_t rsrc(const void* p, unsigned bytes) { return __builtin_amdgcn_make_buffer_rsrc((void*)p, 0, bytes, 0x00020000); }
#define BARRIER() do { asm volatile("s_waitcnt lgkmcnt(0)" ::: "memory"); __builtin_amdgcn_s_barrier(); asm volatile("" ::: "memory"); } while (0)
#define WAITVM(n) asm volatile("s_waitcnt vmcnt(" #n ")" ::: "memory")

__global__ void __launch_bounds__(768) k_main(const u16* __restrict__ q, const u16* __restrict__ f0, const u16* __restrict__ f1,
                                              const u16* __restrict__ wq, const u16* __restrict__ wk, const u16* __restrict__ wv,
                                              const u16* __restrict__ wo, float* __restrict__ part) {
  __shared__ __attribute__((aligned(16))) char lds_raw[L_END];
  const unsigned lds = (unsigned)(size_t)(LAS char*)lds_raw;
  const int lane = threadIdx.x & 63, i = lane & 15, g = lane >> 4;
  const int wave = __builtin_amdgcn_readfirstlane(threadIdx.x >> 6);
  const int b = blockIdx.x;

  if (wave >= 1 && wave <= NSW) {
    const int s = wave - 1;
    const unsigned base = lds + s * REG;
    {
      const unsigned u = NSW * b + s, yp = (u * 43691u) >> 19;
      const unsigned o1 = u * 3072u, o0 = u * 6144u + yp * 73728u;
      const unsigned v0 = (lane * 16) ^ (lane >= 48 ? 32 : 0), v1 = (lane * 16) ^ (lane < 32 ? 32 : 0), v2 = (lane * 16) ^ (lane >= 16 ? 32 : 0);
      const __amdgpu_buffer_rsrc_t r0 = rsrc(f0, 384u * 384u * 192u), r1 = rsrc(f1, 192u * 192u * 192u);
      unsigned keep, so;
#define G3(R) "buffer_load_dwordx4 %2, " R ", %1 offen lds\n\tbuffer_load_dwordx4 %3, " R ", %1 offen offset:1024 lds\n\tbuffer_load_dwordx4 %4, " R ", %1 offen offset:2048 lds\n\t"
      asm volatile(
          "s_mov_b32 %0, m0\n\t"
          "s_mov_b32 m0, %7\n\ts_mov_b32 %1, %9\n\t" G3("%6")
          "s_add_u32 m0, %7, 3072\n\ts_mov_b32 %1, %8\n\t" G3("%5")
          "s_add_u32 m0, %7, 9216\n\ts_add_u32 %1, %8, 73728\n\t" G3("%5")
          "s_add_u32 m0, %7, 6144\n\ts_add_u32 %1, %8, 3072\n\t" G3("%5")
          "s_add_u32 m0, %7, 12288\n\ts_add_u32 %1, %8, 76800\n\t" G3("%5")
          "s_mov_b32 m0, %0"
          : "=&s"(keep), "=&s"(so)
          : "v"(v0), "v"(v1), "v"(v2), "s"(r0), "s"(r1), "s"(base), "s"(o0), "s"(o1)
          : "memory", "scc");
#undef G3
    }
    const unsigned aoff = base + 3072 + 192 * i + 16 * (g ^ (((i >> 2) & 1) << 1));
    const unsigned foff = base + 192 * (i >> 1) + 16 * (g ^ ((i >> 3) << 1));
    const int tq = (lane >> 2) & 3, tp = lane & 3;
    const unsigned tb = base + 3072 + 192 * (4 * g + tq) + 8 * tp;
    const unsigned tE = tb + 32 * (g & 1), tO = tb - 32 * (g & 1);
    const unsigned fb = base + 192 * (8 * (tq >> 1) + 2 * g + (tq & 1)) + 8 * tp;
    const unsigned fE = fb + 32 * (g >> 1), fO = fb - 32 * (g >> 1);
    const u32x4 ones = {0x3f803f80u, 0x3f803f80u, 0x3f803f80u, 0x3f803f80u};

    BARRIER();
    u32x4 qk[3];
#pragma unroll
    for (int k = 0; k < 3; ++k) qk[k] = lds128(lds + L_QK + 1024 * k + 16 * lane);
    const float sq = *(const LAS float*)(lds + L_SQ + 4 * i);

    f32x4 acc[6];
#pragma unroll
    for (int t = 0; t < 6; ++t) acc[t] = f32x4{0.f, 0.f, 0.f, 0.f};
    float m = 0.f, l = 0.f, p1[4];
#pragma unroll
    for (int h = 0; h < 2; ++h) {
      if (h == 0) WAITVM(9); else WAITVM(3);
      u32x4 F[3];
#pragma unroll
      for (int k = 0; k < 3; ++k) F[k] = lds128(foff + 1536 * h + 64 * k);
      f32x4 d1 = {0.f, 0.f, 0.f, 0.f}, s1f = {0.f, 0.f, 0.f, 0.f};
      float ff = 0.f;
#pragma unroll
      for (int k = 0; k < 3; ++k) {
        d1 = mma32(F[k], qk[k], d1);
        s1f = mma32(F[k], ones, s1f);
#pragma unroll
        for (int e = 0; e < 4; ++e) ff = dot2(F[k][e], F[k][e], ff);
      }
      float lg[2][4], rs[2][4];
#pragma unroll
      for (int dy = 0; dy < 2; ++dy) {
        if (dy == 1) { if (h == 0) WAITVM(6); else WAITVM(0); }
        u32x4 X[3];
#pragma unroll
        for (int k = 0; k < 3; ++k) X[k] = lds128(aoff + 6144 * dy + 3072 * h + 64 * k);
        f32x4 dl = d1, s1 = s1f;
        float xx = ff, xf = 0.f;
#pragma unroll
        for (int k = 0; k < 3; ++k) {
          dl = mma32(X[k], qk[k], dl);
          s1 = mma32(X[k], ones, s1);
#pragma unroll
          for (int e = 0; e < 4; ++e) { xx = dot2(X[k][e], X[k][e], xx); xf = dot2(X[k][e], F[k][e], xf); }
        }
        const float c0 = 96.f * EPS;
        const f32x4 s2 = __builtin_amdgcn_mfma_f32_16x16x4f32(fmaf(2.f, xf, xx), 1.0f, f32x4{c0, c0, c0, c0}, 0, 0, 0);
#pragma unroll
        for (int r = 0; r < 4; ++r) {
          const float mean = s1[r] * (1.f / 96.f);
          const float t = fmaxf(fmaf(-mean, s1[r], s2[r]), c0);
          rs[dy][r] = __builtin_amdgcn_rsqf(t);
          lg[dy][r] = fmaf(-mean, sq, dl[r]) * rs[dy][r];
        }
      }
      float mx = fmaxf(fmaxf(fmaxf(lg[0][0], lg[0][1]), fmaxf(lg[0][2], lg[0][3])), fmaxf(fmaxf(lg[1][0], lg[1][1]), fmaxf(lg[1][2], lg[1][3])));
      mx = xg_max(mx);
      if (h == 0) m = mx;
      else {
        const float mn = fmaxf(m, mx), al = __builtin_amdgcn_exp2f(m - mn);
        m = mn; l *= al; p1[0] *= al; p1[1] *= al;
#pragma unroll
        for (int t = 0; t < 6; ++t) acc[t] *= al;
      }
      float pp[2][4];
#pragma unroll
      for (int dy = 0; dy < 2; ++dy)
#pragma unroll
        for (int r = 0; r < 4; ++r) { const float p = __builtin_amdgcn_exp2f(lg[dy][r] - m); l += p; pp[dy][r] = p * rs[dy][r]; }
      p1[2 * h] = (pp[0][0] + pp[0][1]) + (pp[1][0] + pp[1][1]);
      p1[2 * h + 1] = (pp[0][2] + pp[0][3]) + (pp[1][2] + pp[1][3]);
      const u32x4 P = {pk(pp[0][0], pp[0][1]), pk(pp[0][2], pp[0][3]), pk(pp[1][0], pp[1][1]), pk(pp[1][2], pp[1][3])};
#pragma unroll
      for (int t = 0; t < 6; ++t) {
        const unsigned a = ((t & 1) ? tO : tE) + 3072 * h + 32 * t;
        const u32x2 x0 = ldstr(a), x1 = ldstr(a + 6144);
        acc[t] = mma32(u32x4{x0[0], x0[1], x1[0], x1[1]}, P, acc[t]);
      }
    }
    {
      const u32x2 P1 = {pk(p1[0], p1[1]), pk(p1[2], p1[3])};
#pragma unroll
      for (int t = 0; t < 6; ++t) acc[t] = mma16(ldstr(((t & 1) ? fO : fE) + 32 * t), P1, acc[t]);
    }
    l = xg_sum(l);
#pragma unroll
    for (int t = 0; t < 6; ++t) *(LAS f32x4*)(base + 1024 * t + 16 * lane) = acc[t];
    if (g == 0) { *(LAS float*)(lds + L_ML + 128 * s + 4 * i) = m; *(LAS float*)(lds + L_ML + 128 * s + 64 + 4 * i) = l; }
    BARRIER();
    if (s < 6) {
      float mw[NSW], M = -INFINITY;
#pragma unroll
      for (int w = 0; w < NSW; ++w) { mw[w] = *(const LAS float*)(lds + L_ML + 128 * w + 4 * i); M = fmaxf(M, mw[w]); }
      f32x4 S = {0.f, 0.f, 0.f, 0.f};
#pragma unroll
      for (int w = 0; w < NSW; ++w) S += __builtin_amdgcn_exp2f(mw[w] - M) * *(const LAS f32x4*)(lds + w * REG + 1024 * s + 16 * lane);
      *(LAS u32x2*)(lds + L_QK + 1024 * (s >> 1) + 16 * lane + 8 * (s & 1)) = u32x2{pk(S[0], S[1]), pk(S[2], S[3])};
    }
    BARRIER();
  } else if (wave == 0) {
    const __amdgpu_buffer_rsrc_t rq = rsrc(q, 3072), rwq = rsrc(wq, 18432), rwk = rsrc(wk, 18432);
    u32x4 qf[3];
#pragma unroll
    for (int k = 0; k < 3; ++k) qf[k] = __builtin_amdgcn_raw_buffer_load_b128(rq, 192 * i + 16 * g + 64 * k, 0, 0);
    u32x3 raw[24];
#pragma unroll
    for (int k = 0; k < 24; ++k) raw[k] = __builtin_amdgcn_raw_buffer_load_b96(rwq, 1536 * g + 12 * i + 192 * (k & 7), 6144 * (k >> 3), 0);
    {
      unsigned keep, so; const unsigned v = lane * 16, lb = lds + L_WK;
#define G4 "buffer_load_dwordx4 %2, %3, %1 offen lds\n\tbuffer_load_dwordx4 %2, %3, %1 offen offset:1024 lds\n\tbuffer_load_dwordx4 %2, %3, %1 offen offset:2048 lds\n\t"
#define NX(o) "s_add_u32 m0, %4, " #o "\n\ts_mov_b32 %1, " #o "\n\t"
      asm volatile("s_mov_b32 %0, m0\n\t" NX(0) G4 NX(3072) G4 NX(6144) G4 NX(9216) G4 NX(12288) G4 NX(15360) G4 "s_mov_b32 m0, %0"
                   : "=&s"(keep), "=&s"(so) : "v"(v), "s"(rwk), "s"(lb) : "memory", "scc");
#undef G4
#undef NX
    }
    float x[24], sm = 0.f;
#pragma unroll
    for (int k = 0; k < 12; ++k) { const unsigned w = qf[k >> 2][k & 3]; x[2 * k] = bflo(w); x[2 * k + 1] = bfhi(w); sm += x[2 * k] + x[2 * k + 1]; }
    const float mean = xg_sum(sm) * (1.f / 96.f);
    float vs = 0.f;
#pragma unroll
    for (int k = 0; k < 24; ++k) { x[k] -= mean; vs = fmaf(x[k], x[k], vs); }
    const float rstd = __builtin_amdgcn_rsqf(xg_sum(vs) * (1.f / 96.f) + EPS);
    u32x4 qh[3], ql[3];
#pragma unroll
    for (int k = 0; k < 12; ++k) {
      const float a = x[2 * k] * rstd, c = x[2 * k + 1] * rstd; const unsigned h = pk(a, c);
      qh[k >> 2][k & 3] = h; ql[k >> 2][k & 3] = pk(a - bflo(h), c - bfhi(h));
    }
    f32x4 Q[6];
#pragma unroll
    for (int t = 0; t < 6; ++t) {
      Q[t] = f32x4{0.f, 0.f, 0.f, 0.f};
#pragma unroll
      for (int k = 0; k < 3; ++k) {
        u32x4 A;
#pragma unroll
        for (int e = 0; e < 4; ++e) { const unsigned lo = raw[8 * k + 2 * e][t >> 1], hi = raw[8 * k + 2 * e + 1][t >> 1]; A[e] = __builtin_amdgcn_perm(hi, lo, (t & 1) ? 0x07060302u : 0x05040100u); }
        Q[t] = mma32(A, qh[k], Q[t]);
        Q[t] = mma32(A, ql[k], Q[t]);
      }
    }
    u32x4 Qh[3], Ql[3];
#pragma unroll
    for (int k = 0; k < 12; ++k) {
      const float a = Q[(2 * k) % 6][(2 * k) / 6], c = Q[(2 * k + 1) % 6][(2 * k + 1) / 6]; const unsigned h = pk(a, c);
      Qh[k >> 2][k & 3] = h; Ql[k >> 2][k & 3] = pk(a - bflo(h), c - bfhi(h));
    }
    WAITVM(0);
    const unsigned wa = lds + L_WK + 192 * (8 * (i >> 2) + (i & 3)) + 48 * g;
    float sq = 0.f;
#pragma unroll
    for (int k = 0; k < 3; ++k) {
      f32x4 D[2];
#pragma unroll
      for (int h = 0; h < 2; ++h) {
        D[h] = f32x4{0.f, 0.f, 0.f, 0.f};
#pragma unroll
        for (int j = 0; j < 3; ++j) { const u32x4 A = lds128(wa + 6144 * k + 768 * h + 16 * j); D[h] = mma32(A, Qh[j], D[h]); D[h] = mma32(A, Ql[j], D[h]); }
        D[h] *= LOG2E;
      }
      const u32x4 o = {pk(D[0][0], D[0][1]), pk(D[0][2], D[0][3]), pk(D[1][0], D[1][1]), pk(D[1][2], D[1][3])};
      *(LAS u32x4*)(lds + L_QK + 1024 * k + 16 * lane) = o;
#pragma unroll
      for (int e = 0; e < 4; ++e) sq = dot2(o[e], 0x3f803f80u, sq);
    }
    sq = xg_sum(sq);
    if (g == 0) *(LAS float*)(lds + L_SQ + 4 * i) = sq;
    BARRIER();
    BARRIER();
    BARRIER();
  } else {
    const int hh = wave - (NSW + 1);
    const __amdgpu_buffer_rsrc_t rwv = rsrc(wv, 18432), rwo = rsrc(wo, 18432);
    u32x2 raw[24];
#pragma unroll
    for (int k = 0; k < 24; ++k) raw[k] = __builtin_amdgcn_raw_buffer_load_b64(rwo, 1536 * g + 12 * i + 192 * (k & 7), 6144 * (k >> 3) + 4 * hh, 0);
    u32x4 A[18];
#pragma unroll
    for (int k = 0; k < 18; ++k) A[k] = __builtin_amdgcn_raw_buffer_load_b128(rwv, 192 * i + 16 * g + 64 * (k % 3), 3072 * (k / 3), 0);
    u32x4 B[3][3];
#pragma unroll
    for (int k = 0; k < 12; ++k) {
      const unsigned l0 = raw[2 * k][0], l1 = raw[2 * k][1], h0 = raw[2 * k + 1][0], h1 = raw[2 * k + 1][1];
      const unsigned sa = hh ? 0x07060302u : 0x05040100u, sb = hh ? 0x05040100u : 0x07060302u;
      B[0][k >> 2][k & 3] = __builtin_amdgcn_perm(h0, l0, sa);
      B[1][k >> 2][k & 3] = __builtin_amdgcn_perm(hh ? h1 : h0, hh ? l1 : l0, sb);
      B[2][k >> 2][k & 3] = __builtin_amdgcn_perm(h1, l1, sa);
    }
    f32x4 D[3][6];
#pragma unroll
    for (int t = 0; t < 6; ++t)
#pragma unroll
      for (int qq = 0; qq < 3; ++qq) {
        D[qq][t] = f32x4{0.f, 0.f, 0.f, 0.f};
#pragma unroll
        for (int k = 0; k < 3; ++k) D[qq][t] = mma32(A[3 * t + k], B[qq][k], D[qq][t]);
      }
    u32x4 W[3][3];
#pragma unroll
    for (int qq = 0; qq < 3; ++qq) {
      float cs = 0.f;
#pragma unroll
      for (int t = 0; t < 6; ++t) cs += (D[qq][t][0] + D[qq][t][1]) + (D[qq][t][2] + D[qq][t][3]);
      const float sc = 9.797958971132712f, mu = xg_sum(cs) * (sc / 96.f);
#pragma unroll
      for (int t = 0; t < 6; ++t) {
        W[qq][t >> 1][2 * (t & 1)] = pk(fmaf(D[qq][t][0], sc, -mu), fmaf(D[qq][t][1], sc, -mu));
        W[qq][t >> 1][2 * (t & 1) + 1] = pk(fmaf(D[qq][t][2], sc, -mu), fmaf(D[qq][t][3], sc, -mu));
      }
    }
    BARRIER();
    BARRIER();
    float mw[NSW], M = -INFINITY, L = 0.f;
#pragma unroll
    for (int w = 0; w < NSW; ++w) { mw[w] = *(const LAS float*)(lds + L_ML + 128 * w + 4 * i); M = fmaxf(M, mw[w]); }
#pragma unroll
    for (int w = 0; w < NSW; ++w) L = fmaf(__builtin_amdgcn_exp2f(mw[w] - M), *(const LAS float*)(lds + L_ML + 128 * w + 64 + 4 * i), L);
    BARRIER();
    u32x4 S[3];
#pragma unroll
    for (int k = 0; k < 3; ++k) S[k] = lds128(lds + L_QK + 1024 * k + 16 * lane);
    float* po = part + (size_t)(b * 16 + i) * 100;
#pragma unroll
    for (int qq = 0; qq < 3; ++qq) {
      f32x4 T = {0.f, 0.f, 0.f, 0.f};
#pragma unroll
      for (int k = 0; k < 3; ++k) T = mma32(W[qq][k], S[k], T);
#pragma unroll
      for (int r = 0; r < 4; ++r) po[24 * g + 6 * r + 3 * hh + qq] = T[r];
    }
    if (g == 0 && hh == 0) { po[96] = M; po[97] = L; }
  }
}

__global__ void __launch_bounds__(768) k_fin(const u16* __restrict__ q, const float* __restrict__ part, u16* __restrict__ out) {
  __shared__ float sT[8][96], sM[8], sL[8];
  const int n = blockIdx.x, e = threadIdx.x % 96, gr = threadIdx.x / 96;
  const float* p = part + (size_t)(gr * 32 * 16 + n) * 100;
  float mv[32], lv[32], tv[32];
#pragma unroll
  for (int k = 0; k < 32; ++k) { mv[k] = p[k * 1600 + 96]; lv[k] = p[k * 1600 + 97]; tv[k] = p[k * 1600 + e]; }
  float M = -INFINITY;
#pragma unroll
  for (int k = 0; k < 32; ++k) M = fmaxf(M, mv[k]);
  float L = 0.f, T = 0.f;
#pragma unroll
  for (int k = 0; k < 32; ++k) { const float w = __builtin_amdgcn_exp2f(mv[k] - M); L = fmaf(w, lv[k], L); T = fmaf(w, tv[k], T); }
  sT[gr][e] = T; if (e == 0) { sM[gr] = M; sL[gr] = L; }
  __syncthreads();
  if (gr == 0) {
    float MM = -INFINITY;
#pragma unroll
    for (int k = 0; k < 8; ++k) MM = fmaxf(MM, sM[k]);
    float LL = 0.f, TT = 0.f;
#pragma unroll
    for (int k = 0; k < 8; ++k) { const float w = __builtin_amdgcn_exp2f(sM[k] - MM); LL = fmaf(w, sL[k], LL); TT = fmaf(w, sT[k][e], TT); }
    const float o = __uint_as_float((unsigned)q[n * 96 + e] << 16) + TT / LL;
    out[n * 96 + e] = (u16)(pk(o, o) & 0xffff);
  }
}

extern "C" void kernel_launch(void* const* d_in, const int* in_sizes, int n_in, void* d_out, int out_size, void* d_ws, size_t ws_size,
                              hipStream_t stream) {
  const u16* q = (const u16*)d_in[0]; const u16* f0 = (const u16*)d_in[1]; const u16* f1 = (const u16*)d_in[2];
  const u16* wq = (const u16*)d_in[3]; const u16* wk = (const u16*)d_in[4]; const u16* wv = (const u16*)d_in[5]; const u16* wo = (const u16*)d_in[6];
  float* part = (float*)d_ws;
  k_main<<<NWG, 768, 0, stream>>>(q, f0, f1, wq, wk, wv, wo, part);
  k_fin<<<16, 768, 0, stream>>>(q, part, (u16*)d_out);
}
```

```cpp
#define AUX 0
#define STAT 5
#include <hip/hip_runtime.h>
#include <hip/hip_bf16.h>
#pragma clang diagnostic ignored "-Wint-to-pointer-cast"

typedef unsigned short u16;
typedef __attribute__((ext_vector_type(4))) short s16x4;
typedef __attribute__((ext_vector_type(8))) __bf16 bf16x8;
typedef __attribute__((ext_vector_type(2))) __bf16 bf16x2;
typedef __attribute__((ext_vector_type(2))) float f32x2;
typedef __attribute__((ext_vector_type(4))) float f32x4;
typedef __attribute__((ext_vector_type(4))) unsigned u32x4;
typedef __attribute__((ext_vector_type(3))) unsigned u32x3;
typedef __attribute__((ext_vector_type(2))) unsigned u32x2;
#define LAS __attribute__((address_space(3)))
#define DEVI __device__ __forceinline__

constexpr int NWG = 256;
constexpr int NSW = 9;
constexpr int REG = 15360;
constexpr int L_WK = NSW * REG;
constexpr int L_QK = L_WK + 18432;
constexpr int L_SQ = L_QK + 3072;
constexpr int L_ML = L_SQ + 64;
constexpr int L_END = L_ML + NSW * 128;
constexpr float EPS = 1e-5f;
constexpr float LOG2E = 1.4426950408889634f;

DEVI float bflo(unsigned x) { return __uint_as_float(x << 16); }
DEVI float bfhi(unsigned x) { return __uint_as_float(x & 0xffff0000u); }
DEVI unsigned pk(float a, float b) { f32x2 p = {a, b}; bf16x2 h = __builtin_convertvector(p, bf16x2); return __builtin_bit_cast(unsigned, h); }
DEVI float dot2(unsigned a, unsigned b, float c) { return __builtin_amdgcn_fdot2_f32_bf16(__builtin_bit_cast(bf16x2, a), __builtin_bit_cast(bf16x2, b), c, false); }
DEVI f32x4 mma32(u32x4 a, u32x4 b, f32x4 c) { return __builtin_amdgcn_mfma_f32_16x16x32_bf16(__builtin_bit_cast(bf16x8, a), __builtin_bit_cast(bf16x8, b), c, 0, 0, 0); }
DEVI f32x4 mma16(u32x2 a, u32x2 b, f32x4 c) { return __builtin_amdgcn_mfma_f32_16x16x16bf16_1k(__builtin_bit_cast(s16x4, a), __builtin_bit_cast(s16x4, b), c, 0, 0, 0); }
DEVI float xg_sum(float x) {
  auto s = __builtin_amdgcn_permlane16_swap(__float_as_uint(x), __float_as_uint(x), false, false);
  x = __uint_as_float(s[0]) + __uint_as_float(s[1]);
  auto t = __builtin_amdgcn_permlane32_swap(__float_as_uint(x), __float_as_uint(x), false, false);
  return __uint_as_float(t[0]) + __uint_as_float(t[1]);
}
DEVI float xg_max(float x) {
  auto s = __builtin_amdgcn_permlane16_swap(__float_as_uint(x), __float_as_uint(x), false, false);
  x = fmaxf(__uint_as_float(s[0]), __uint_as_float(s[1]));
  auto t = __builtin_amdgcn_permlane32_swap(__float_as_uint(x), __float_as_uint(x), false, false);
  return fmaxf(__uint_as_float(t[0]), __uint_as_float(t[1]));
}
DEVI u32x4 lds128(unsigned a) { return *(const LAS u32x4*)a; }
DEVI u32x2 ldstr(unsigned a) { s16x4 t = __builtin_amdgcn_ds_read_tr16_b64_v4i16((LAS s16x4*)a); return __builtin_bit_cast(u32x2, t); }
DEVI __amdgpu_buffer_rsrc_t rsrc(const void* p, unsigned bytes) { return __builtin_amdgcn_make_buffer_rsrc((void*)p, 0, bytes, 0x00020000); }
#define BARRIER() do { asm volatile("s_waitcnt lgkmcnt(0)" ::: "memory"); __builtin_amdgcn_s_barrier(); asm volatile("" ::: "memory"); } while (0)
#define WAITVM(n) asm volatile("s_waitcnt vmcnt(" #n ")" ::: "memory")

__global__ void __launch_bounds__(768) k_main(const u16* __restrict__ q, const u16* __restrict__ f0, const u16* __restrict__ f1,
                                              const u16* __restrict__ wq, const u16* __restrict__ wk, const u16* __restrict__ wv,
                                              const u16* __restrict__ wo, float* __restrict__ part) {
  __shared__ __attribute__((aligned(16))) char lds_raw[L_END];
  const unsigned lds = (unsigned)(size_t)(LAS char*)lds_raw;
  const int lane = threadIdx.x & 63, i = lane & 15, g = lane >> 4;
  const int wave = __builtin_amdgcn_readfirstlane(threadIdx.x >> 6);
  const int b = blockIdx.x;

  if (wave >= 1 && wave <= NSW) {
    const int s = wave - 1;
    const unsigned base = lds + s * REG;
    {
      const unsigned u = NSW * b + s, yp = (u * 43691u) >> 19;
      const unsigned o1 = u * 3072u, o0 = u * 6144u + yp * 73728u;
      const unsigned v0 = (lane * 16) ^ (lane >= 48 ? 32 : 0), v1 = (lane * 16) ^ (lane < 32 ? 32 : 0), v2 = (lane * 16) ^ (lane >= 16 ? 32 : 0);
      const __amdgpu_buffer_rsrc_t r0 = rsrc(f0, 384u * 384u * 192u), r1 = rsrc(f1, 192u * 192u * 192u);
      unsigned keep, so;
#define G3(R) "buffer_load_dwordx4 %2, " R ", %1 offen nt lds\n\tbuffer_load_dwordx4 %3, " R ", %1 offen offset:1024 nt lds\n\tbuffer_load_dwordx4 %4, " R ", %1 offen offset:2048 nt lds\n\t"
      asm volatile(
          "s_mov_b32 %0, m0\n\t"
          "s_mov_b32 m0, %7\n\ts_mov_b32 %1, %9\n\t" G3("%6")
          "s_add_u32 m0, %7, 3072\n\ts_mov_b32 %1, %8\n\t" G3("%5")
          "s_add_u32 m0, %7, 9216\n\ts_add_u32 %1, %8, 73728\n\t" G3("%5")
          "s_add_u32 m0, %7, 6144\n\ts_add_u32 %1, %8, 3072\n\t" G3("%5")
          "s_add_u32 m0, %7, 12288\n\ts_add_u32 %1, %8, 76800\n\t" G3("%5")
          "s_mov_b32 m0, %0"
          : "=&s"(keep), "=&s"(so)
          : "v"(v0), "v"(v1), "v"(v2), "s"(r0), "s"(r1), "s"(base), "s"(o0), "s"(o1)
          : "memory", "scc");
#undef G3
    }
    const unsigned aoff = base + 3072 + 192 * i + 16 * (g ^ (((i >> 2) & 1) << 1));
    const unsigned foff = base + 192 * (i >> 1) + 16 * (g ^ ((i >> 3) << 1));
    const int tq = (lane >> 2) & 3, tp = lane & 3;
    const unsigned tb = base + 3072 + 192 * (4 * g + tq) + 8 * tp;
    const unsigned tE = tb + 32 * (g & 1), tO = tb - 32 * (g & 1);
    const unsigned fb = base + 192 * (8 * (tq >> 1) + 2 * g + (tq & 1)) + 8 * tp;
    const unsigned fE = fb + 32 * (g >> 1), fO = fb - 32 * (g >> 1);
    const u32x4 ones = {0x3f803f80u, 0x3f803f80u, 0x3f803f80u, 0x3f803f80u};

    BARRIER();
    u32x4 qk[3];
#pragma unroll
    for (int k = 0; k < 3; ++k) qk[k] = lds128(lds + L_QK + 1024 * k + 16 * lane);
    const float sq = *(const LAS float*)(lds + L_SQ + 4 * i);

    f32x4 acc[6];
#pragma unroll
    for (int t = 0; t < 6; ++t) acc[t] = f32x4{0.f, 0.f, 0.f, 0.f};
    float m = 0.f, l = 0.f, p1[4];
#pragma unroll
    for (int h = 0; h < 2; ++h) {
      if (h == 0) WAITVM(9); else WAITVM(3);
      u32x4 F[3];
#pragma unroll
      for (int k = 0; k < 3; ++k) F[k] = lds128(foff + 1536 * h + 64 * k);
      f32x4 d1 = {0.f, 0.f, 0.f, 0.f}, s1f = {0.f, 0.f, 0.f, 0.f};
      float ff = 0.f;
#pragma unroll
      for (int k = 0; k < 3; ++k) {
        d1 = mma32(F[k], qk[k], d1);
        s1f = mma32(F[k], ones, s1f);
#pragma unroll
        for (int e = 0; e < 4; ++e) ff = dot2(F[k][e], F[k][e], ff);
      }
      float lg[2][4], rs[2][4];
#pragma unroll
      for (int dy = 0; dy < 2; ++dy) {
        if (dy == 1) { if (h == 0) WAITVM(6); else WAITVM(0); }
        u32x4 X[3];
#pragma unroll
        for (int k = 0; k < 3; ++k) X[k] = lds128(aoff + 6144 * dy + 3072 * h + 64 * k);
        f32x4 dl = d1, s1 = s1f;
        float xx = ff, xf = 0.f;
#pragma unroll
        for (int k = 0; k < 3; ++k) {
          dl = mma32(X[k], qk[k], dl);
          s1 = mma32(X[k], ones, s1);
#pragma unroll
          for (int e = 0; e < 4; ++e) { xx = dot2(X[k][e], X[k][e], xx); xf = dot2(X[k][e], F[k][e], xf); }
        }
        const float c0 = 96.f * EPS;
        const f32x4 s2 = __builtin_amdgcn_mfma_f32_16x16x4f32(fmaf(2.f, xf, xx), 1.0f, f32x4{c0, c0, c0, c0}, 0, 0, 0);
#pragma unroll
        for (int r = 0; r < 4; ++r) {
          const float mean = s1[r] * (1.f / 96.f);
          const float t = fmaxf(fmaf(-mean, s1[r], s2[r]), c0);
          rs[dy][r] = __builtin_amdgcn_rsqf(t);
          lg[dy][r] = fmaf(-mean, sq, dl[r]) * rs[dy][r];
        }
      }
      float mx = fmaxf(fmaxf(fmaxf(lg[0][0], lg[0][1]), fmaxf(lg[0][2], lg[0][3])), fmaxf(fmaxf(lg[1][0], lg[1][1]), fmaxf(lg[1][2], lg[1][3])));
      mx = xg_max(mx);
      if (h == 0) m = mx;
      else {
        const float mn = fmaxf(m, mx), al = __builtin_amdgcn_exp2f(m - mn);
        m = mn; l *= al; p1[0] *= al; p1[1] *= al;
#pragma unroll
        for (int t = 0; t < 6; ++t) acc[t] *= al;
      }
      float pp[2][4];
#pragma unroll
      for (int dy = 0; dy < 2; ++dy)
#pragma unroll
        for (int r = 0; r < 4; ++r) { const float p = __builtin_amdgcn_exp2f(lg[dy][r] - m); l += p; pp[dy][r] = p * rs[dy][r]; }
      p1[2 * h] = (pp[0][0] + pp[0][1]) + (pp[1][0] + pp[1][1]);
      p1[2 * h + 1] = (pp[0][2] + pp[0][3]) + (pp[1][2] + pp[1][3]);
      const u32x4 P = {pk(pp[0][0], pp[0][1]), pk(pp[0][2], pp[0][3]), pk(pp[1][0], pp[1][1]), pk(pp[1][2], pp[1][3])};
#pragma unroll
      for (int t = 0; t < 6; ++t) {
        const unsigned a = ((t & 1) ? tO : tE) + 3072 * h + 32 * t;
        const u32x2 x0 = ldstr(a), x1 = ldstr(a + 6144);
        acc[t] = mma32(u32x4{x0[0], x0[1], x1[0], x1[1]}, P, acc[t]);
      }
    }
    {
      const u32x2 P1 = {pk(p1[0], p1[1]), pk(p1[2], p1[3])};
#pragma unroll
      for (int t = 0; t < 6; ++t) acc[t] = mma16(ldstr(((t & 1) ? fO : fE) + 32 * t), P1, acc[t]);
    }
    l = xg_sum(l);
#pragma unroll
    for (int t = 0; t < 6; ++t) *(LAS f32x4*)(base + 1024 * t + 16 * lane) = acc[t];
    if (g == 0) { *(LAS float*)(lds + L_ML + 128 * s + 4 * i) = m; *(LAS float*)(lds + L_ML + 128 * s + 64 + 4 * i) = l; }
    BARRIER();
    if (s < 6) {
      float mw[NSW], M = -INFINITY;
#pragma unroll
      for (int w = 0; w < NSW; ++w) { mw[w] = *(const LAS float*)(lds + L_ML + 128 * w + 4 * i); M = fmaxf(M, mw[w]); }
      f32x4 S = {0.f, 0.f, 0.f, 0.f};
#pragma unroll
      for (int w = 0; w < NSW; ++w) S += __builtin_amdgcn_exp2f(mw[w] - M) * *(const LAS f32x4*)(lds + w * REG + 1024 * s + 16 * lane);
      *(LAS u32x2*)(lds + L_QK + 1024 * (s >> 1) + 16 * lane + 8 * (s & 1)) = u32x2{pk(S[0], S[1]), pk(S[2], S[3])};
    }
    BARRIER();
  } else if (wave == 0) {
    const __amdgpu_buffer_rsrc_t rq = rsrc(q, 3072), rwq = rsrc(wq, 18432), rwk = rsrc(wk, 18432);
    u32x4 qf[3];
#pragma unroll
    for (int k = 0; k < 3; ++k) qf[k] = __builtin_amdgcn_raw_buffer_load_b128(rq, 192 * i + 16 * g + 64 * k, 0, 0);
    u32x3 raw[24];
#pragma unroll
    for (int k = 0; k < 24; ++k) raw[k] = __builtin_amdgcn_raw_buffer_load_b96(rwq, 1536 * g + 12 * i + 192 * (k & 7), 6144 * (k >> 3), 0);
    {
      unsigned keep, so; const unsigned v = lane * 16, lb = lds + L_WK;
#define G4 "buffer_load_dwordx4 %2, %3, %1 offen lds\n\tbuffer_load_dwordx4 %2, %3, %1 offen offset:1024 lds\n\tbuffer_load_dwordx4 %2, %3, %1 offen offset:2048 lds\n\t"
#define NX(o) "s_add_u32 m0, %4, " #o "\n\ts_mov_b32 %1, " #o "\n\t"
      asm volatile("s_mov_b32 %0, m0\n\t" NX(0) G4 NX(3072) G4 NX(6144) G4 NX(9216) G4 NX(12288) G4 NX(15360) G4 "s_mov_b32 m0, %0"
                   : "=&s"(keep), "=&s"(so) : "v"(v), "s"(rwk), "s"(lb) : "memory", "scc");
#undef G4
#undef NX
    }
    float x[24], sm = 0.f;
#pragma unroll
    for (int k = 0; k < 12; ++k) { const unsigned w = qf[k >> 2][k & 3]; x[2 * k] = bflo(w); x[2 * k + 1] = bfhi(w); sm += x[2 * k] + x[2 * k + 1]; }
    const float mean = xg_sum(sm) * (1.f / 96.f);
    float vs = 0.f;
#pragma unroll
    for (int k = 0; k < 24; ++k) { x[k] -= mean; vs = fmaf(x[k], x[k], vs); }
    const float rstd = __builtin_amdgcn_rsqf(xg_sum(vs) * (1.f / 96.f) + EPS);
    u32x4 qh[3], ql[3];
#pragma unroll
    for (int k = 0; k < 12; ++k) {
      const float a = x[2 * k] * rstd, c = x[2 * k + 1] * rstd; const unsigned h = pk(a, c);
      qh[k >> 2][k & 3] = h; ql[k >> 2][k & 3] = pk(a - bflo(h), c - bfhi(h));
    }
    f32x4 Q[6];
#pragma unroll
    for (int t = 0; t < 6; ++t) {
      Q[t] = f32x4{0.f, 0.f, 0.f, 0.f};
#pragma unroll
      for (int k = 0; k < 3; ++k) {
        u32x4 A;
#pragma unroll
        for (int e = 0; e < 4; ++e) { const unsigned lo = raw[8 * k + 2 * e][t >> 1], hi = raw[8 * k + 2 * e + 1][t >> 1]; A[e] = __builtin_amdgcn_perm(hi, lo, (t & 1) ? 0x07060302u : 0x05040100u); }
        Q[t] = mma32(A, qh[k], Q[t]);
        Q[t] = mma32(A, ql[k], Q[t]);
      }
    }
    u32x4 Qh[3], Ql[3];
#pragma unroll
    for (int k = 0; k < 12; ++k) {
      const float a = Q[(2 * k) % 6][(2 * k) / 6], c = Q[(2 * k + 1) % 6][(2 * k + 1) / 6]; const unsigned h = pk(a, c);
      Qh[k >> 2][k & 3] = h; Ql[k >> 2][k & 3] = pk(a - bflo(h), c - bfhi(h));
    }
    WAITVM(0);
    const unsigned wa = lds + L_WK + 192 * (8 * (i >> 2) + (i & 3)) + 48 * g;
    float sq = 0.f;
#pragma unroll
    for (int k = 0; k < 3; ++k) {
      f32x4 D[2];
#pragma unroll
      for (int h = 0; h < 2; ++h) {
        D[h] = f32x4{0.f, 0.f, 0.f, 0.f};
#pragma unroll
        for (int j = 0; j < 3; ++j) { const u32x4 A = lds128(wa + 6144 * k + 768 * h + 16 * j); D[h] = mma32(A, Qh[j], D[h]); D[h] = mma32(A, Ql[j], D[h]); }
        D[h] *= LOG2E;
      }
      const u32x4 o = {pk(D[0][0], D[0][1]), pk(D[0][2], D[0][3]), pk(D[1][0], D[1][1]), pk(D[1][2], D[1][3])};
      *(LAS u32x4*)(lds + L_QK + 1024 * k + 16 * lane) = o;
#pragma unroll
      for (int e = 0; e < 4; ++e) sq = dot2(o[e], 0x3f803f80u, sq);
    }
    sq = xg_sum(sq);
    if (g == 0) *(LAS float*)(lds + L_SQ + 4 * i) = sq;
    BARRIER();
    BARRIER();
    BARRIER();
  } else {
    const int hh = wave - (NSW + 1);
    const __amdgpu_buffer_rsrc_t rwv = rsrc(wv, 18432), rwo = rsrc(wo, 18432);
    u32x2 raw[24];
#pragma unroll
    for (int k = 0; k < 24; ++k) raw[k] = __builtin_amdgcn_raw_buffer_load_b64(rwo, 1536 * g + 12 * i + 192 * (k & 7), 6144 * (k >> 3) + 4 * hh, 0);
    u32x4 A[18];
#pragma unroll
    for (int k = 0; k < 18; ++k) A[k] = __builtin_amdgcn_raw_buffer_load_b128(rwv, 192 * i + 16 * g + 64 * (k % 3), 3072 * (k / 3), 0);
    u32x4 B[3][3];
#pragma unroll
    for (int k = 0; k < 12; ++k) {
      const unsigned l0 = raw[2 * k][0], l1 = raw[2 * k][1], h0 = raw[2 * k + 1][0], h1 = raw[2 * k + 1][1];
      const unsigned sa = hh ? 0x07060302u : 0x05040100u, sb = hh ? 0x05040100u : 0x07060302u;
      B[0][k >> 2][k & 3] = __builtin_amdgcn_perm(h0, l0, sa);
      B[1][k >> 2][k & 3] = __builtin_amdgcn_perm(hh ? h1 : h0, hh ? l1 : l0, sb);
      B[2][k >> 2][k & 3] = __builtin_amdgcn_perm(h1, l1, sa);
    }
    f32x4 D[3][6];
#pragma unroll
    for (int t = 0; t < 6; ++t)
#pragma unroll
      for (int qq = 0; qq < 3; ++qq) {
        D[qq][t] = f32x4{0.f, 0.f, 0.f, 0.f};
#pragma unroll
        for (int k = 0; k < 3; ++k) D[qq][t] = mma32(A[3 * t + k], B[qq][k], D[qq][t]);
      }
    u32x4 W[3][3];
#pragma unroll
    for (int qq = 0; qq < 3; ++qq) {
      float cs = 0.f;
#pragma unroll
      for (int t = 0; t < 6; ++t) cs += (D[qq][t][0] + D[qq][t][1]) + (D[qq][t][2] + D[qq][t][3]);
      const float sc = 9.797958971132712f, mu = xg_sum(cs) * (sc / 96.f);
#pragma unroll
      for (int t = 0; t < 6; ++t) {
        W[qq][t >> 1][2 * (t & 1)] = pk(fmaf(D[qq][t][0], sc, -mu), fmaf(D[qq][t][1], sc, -mu));
        W[qq][t >> 1][2 * (t & 1) + 1] = pk(fmaf(D[qq][t][2], sc, -mu), fmaf(D[qq][t][3], sc, -mu));
      }
    }
    BARRIER();
    BARRIER();
    float mw[NSW], M = -INFINITY, L = 0.f;
#pragma unroll
    for (int w = 0; w < NSW; ++w) { mw[w] = *(const LAS float*)(lds + L_ML + 128 * w + 4 * i); M = fmaxf(M, mw[w]); }
#pragma unroll
    for (int w = 0; w < NSW; ++w) L = fmaf(__builtin_amdgcn_exp2f(mw[w] - M), *(const LAS float*)(lds + L_ML + 128 * w + 64 + 4 * i), L);
    BARRIER();
    u32x4 S[3];
#pragma unroll
    for (int k = 0; k < 3; ++k) S[k] = lds128(lds + L_QK + 1024 * k + 16 * lane);
    float* po = part + (size_t)(b * 16 + i) * 100;
#pragma unroll
    for (int qq = 0; qq < 3; ++qq) {
      f32x4 T = {0.f, 0.f, 0.f, 0.f};
#pragma unroll
      for (int k = 0; k < 3; ++k) T = mma32(W[qq][k], S[k], T);
#pragma unroll
      for (int r = 0; r < 4; ++r) po[24 * g + 6 * r + 3 * hh + qq] = T[r];
    }
    if (g == 0 && hh == 0) { po[96] = M; po[97] = L; }
  }
}

__global__ void __launch_bounds__(768) k_fin(const u16* __restrict__ q, const float* __restrict__ part, u16* __restrict__ out) {
  __shared__ float sT[8][96], sM[8], sL[8];
  const int n = blockIdx.x, e = threadIdx.x % 96, gr = threadIdx.x / 96;
  const float* p = part + (size_t)(gr * 32 * 16 + n) * 100;
  float mv[32], lv[32], tv[32];
#pragma unroll
  for (int k = 0; k < 32; ++k) { mv[k] = p[k * 1600 + 96]; lv[k] = p[k * 1600 + 97]; tv[k] = p[k * 1600 + e]; }
  float M = -INFINITY;
#pragma unroll
  for (int k = 0; k < 32; ++k) M = fmaxf(M, mv[k]);
  float L = 0.f, T = 0.f;
#pragma unroll
  for (int k = 0; k < 32; ++k) { const float w = __builtin_amdgcn_exp2f(mv[k] - M); L = fmaf(w, lv[k], L); T = fmaf(w, tv[k], T); }
  sT[gr][e] = T; if (e == 0) { sM[gr] = M; sL[gr] = L; }
  __syncthreads();
  if (gr == 0) {
    float MM = -INFINITY;
#pragma unroll
    for (int k = 0; k < 8; ++k) MM = fmaxf(MM, sM[k]);
    float LL = 0.f, TT = 0.f;
#pragma unroll
    for (int k = 0; k < 8; ++k) { const float w = __builtin_amdgcn_exp2f(sM[k] - MM); LL = fmaf(w, sL[k], LL); TT = fmaf(w, sT[k][e], TT); }
    const float o = __uint_as_float((unsigned)q[n * 96 + e] << 16) + TT / LL;
    out[n * 96 + e] = (u16)(pk(o, o) & 0xffff);
  }
}


#define GAS __attribute__((address_space(1)))
DEVI unsigned long long rt() { return __builtin_amdgcn_s_memrealtime(); }
__global__ void __launch_bounds__(576) k_diag(const char* f0, const char* f1, unsigned long long* ts) {
  __shared__ __attribute__((aligned(16))) char dl[138240];
  unsigned long long t0 = rt();
  const int wave = __builtin_amdgcn_readfirstlane(threadIdx.x >> 6), lane = threadIdx.x & 63, b = blockIdx.x;
  const char* p0 = f0 + (size_t)b * 110592 + wave * 12288 + lane * 16; const char* p1 = f1 + (size_t)b * 27648 + wave * 3072 + lane * 16;
#pragma unroll
  for (int i = 0; i < 15; i++) {
    const char* a = i < 3 ? p1 + i * 1024 : p0 + (i - 3) * 1024;
    __builtin_amdgcn_global_load_lds((const GAS unsigned*)a, (LAS unsigned*)(dl + (wave * 15 + i) * 1024), 16, 0, AUX);
  }
  asm volatile("s_waitcnt vmcnt(14)" ::: "memory");
  unsigned long long t2 = rt();
  asm volatile("s_waitcnt vmcnt(0)" ::: "memory");
  unsigned acc = *(unsigned*)(dl + wave * 1024 + lane * 4);
  unsigned long long t1 = rt();
  if (acc == 0x12345678) t1++;
  if (lane == 0) { ts[(b * 16 + wave) * 4] = t0; ts[(b * 16 + wave) * 4 + 1] = t1; ts[(b * 16 + wave) * 4 + 2] = t2; }
}
__global__ void __launch_bounds__(64) k_spin(const unsigned long long* ts) {
  unsigned long long mn0 = ~0ull, mx0 = 0, mx1 = 0, s2 = 0, mn1 = ~0ull, s1 = 0;
  for (int b = 0; b < 256; ++b) { unsigned long long e = 0;
    for (int w = 0; w < 9; ++w) { const unsigned long long* t = ts + (b * 16 + w) * 4; mn0 = min(mn0, t[0]); mx0 = max(mx0, t[0]); e = max(e, t[1]); if (w == 0) s2 += t[2] - t[0]; }
    mx1 = max(mx1, e); mn1 = min(mn1, e); s1 += e; }
  unsigned long long st = STAT == 0 ? mx1 - mn0 : STAT == 1 ? mx0 - mn0 : STAT == 2 ? s2 / 256 : STAT == 3 ? mn1 - mn0 : STAT == 4 ? s1 / 256 - mn0 : 0;
  const unsigned long long until = rt() + 20 * st;
  while (rt() < until) ;
}

extern "C" void kernel_launch(void* const* d_in, const int* in_sizes, int n_in, void* d_out, int out_size, void* d_ws, size_t ws_size,
                              hipStream_t stream) {
  const u16* q = (const u16*)d_in[0]; const u16* f0 = (const u16*)d_in[1]; const u16* f1 = (const u16*)d_in[2];
  const u16* wq = (const u16*)d_in[3]; const u16* wk = (const u16*)d_in[4]; const u16* wv = (const u16*)d_in[5]; const u16* wo = (const u16*)d_in[6];
  float* part = (float*)d_ws;
  unsigned long long* ts = (unsigned long long*)((char*)d_ws + (64u << 20));
  k_diag<<<256, 576, 0, stream>>>((const char*)f0, (const char*)f1, ts);
  k_main<<<NWG, 768, 0, stream>>>(q, f0, f1, wq, wk, wv, wo, part);
  k_fin<<<16, 768, 0, stream>>>(q, part, (u16*)d_out);
  k_spin<<<1, 64, 0, stream>>>(ts);
}
```

```cpp
#define AUX 2
#define STAT 5
#include <hip/hip_runtime.h>
#include <hip/hip_bf16.h>
#pragma clang diagnostic ignored "-Wint-to-pointer-cast"

typedef unsigned short u16;
typedef __attribute__((ext_vector_type(4))) short s16x4;
typedef __attribute__((ext_vector_type(8))) __bf16 bf16x8;
typedef __attribute__((ext_vector_type(2))) __bf16 bf16x2;
typedef __attribute__((ext_vector_type(2))) float f32x2;
typedef __attribute__((ext_vector_type(4))) float f32x4;
typedef __attribute__((ext_vector_type(4))) unsigned u32x4;
typedef __attribute__((ext_vector_type(3))) unsigned u32x3;
typedef __attribute__((ext_vector_type(2))) unsigned u32x2;
#define LAS __attribute__((address_space(3)))
#define DEVI __device__ __forceinline__

constexpr int NWG = 256;
constexpr int NSW = 9;
constexpr int REG = 15360;
constexpr int L_WK = NSW * REG;
constexpr int L_QK = L_WK + 18432;
constexpr int L_SQ = L_QK + 3072;
constexpr int L_ML = L_SQ + 64;
constexpr int L_END = L_ML + NSW * 128;
constexpr float EPS = 1e-5f;
constexpr float LOG2E = 1.4426950408889634f;

DEVI float bflo(unsigned x) { return __uint_as_float(x << 16); }
DEVI float bfhi(unsigned x) { return __uint_as_float(x & 0xffff0000u); }
DEVI unsigned pk(float a, float b) { f32x2 p = {a, b}; bf16x2 h = __builtin_convertvector(p, bf16x2); return __builtin_bit_cast(unsigned, h); }
DEVI float dot2(unsigned a, unsigned b, float c) { return __builtin_amdgcn_fdot2_f32_bf16(__builtin_bit_cast(bf16x2, a), __builtin_bit_cast(bf16x2, b), c, false); }
DEVI f32x4 mma32(u32x4 a, u32x4 b, f32x4 c) { return __builtin_amdgcn_mfma_f32_16x16x32_bf16(__builtin_bit_cast(bf16x8, a), __builtin_bit_cast(bf16x8, b), c, 0, 0, 0); }
DEVI f32x4 mma16(u32x2 a, u32x2 b, f32x4 c) { return __builtin_amdgcn_mfma_f32_16x16x16bf16_1k(__builtin_bit_cast(s16x4, a), __builtin_bit_cast(s16x4, b), c, 0, 0, 0); }
DEVI float xg_sum(float x) {
  auto s = __builtin_amdgcn_permlane16_swap(__float_as_uint(x), __float_as_uint(x), false, false);
  x = __uint_as_float(s[0]) + __uint_as_float(s[1]);
  auto t = __builtin_amdgcn_permlane32_swap(__float_as_uint(x), __float_as_uint(x), false, false);
  return __uint_as_float(t[0]) + __uint_as_float(t[1]);
}
DEVI float xg_max(float x) {
  auto s = __builtin_amdgcn_permlane16_swap(__float_as_uint(x), __float_as_uint(x), false, false);
  x = fmaxf(__uint_as_float(s[0]), __uint_as_float(s[1]));
  auto t = __builtin_amdgcn_permlane32_swap(__float_as_uint(x), __float_as_uint(x), false, false);
  return fmaxf(__uint_as_float(t[0]), __uint_as_float(t[1]));
}
DEVI u32x4 lds128(unsigned a) { return *(const LAS u32x4*)a; }
DEVI u32x2 ldstr(unsigned a) { s16x4 t = __builtin_amdgcn_ds_read_tr16_b64_v4i16((LAS s16x4*)a); return __builtin_bit_cast(u32x2, t); }
DEVI __amdgpu_buffer_rsrc_t rsrc(const void* p, unsigned bytes) { return __builtin_amdgcn_make_buffer_rsrc((void*)p, 0, bytes, 0x00020000); }
#define BARRIER() do { asm volatile("s_waitcnt lgkmcnt(0)" ::: "memory"); __builtin_amdgcn_s_barrier(); asm volatile("" ::: "memory"); } while (0)
#define WAITVM(n) asm volatile("s_waitcnt vmcnt(" #n ")" ::: "memory")

__global__ void __launch_bounds__(768) k_main(const u16* __restrict__ q, const u16* __restrict__ f0, const u16* __restrict__ f1,
                                              const u16* __restrict__ wq, const u16* __restrict__ wk, const u16* __restrict__ wv,
                                              const u16* __restrict__ wo, float* __restrict__ part) {
  __shared__ __attribute__((aligned(16))) char lds_raw[L_END];
  const unsigned lds = (unsigned)(size_t)(LAS char*)lds_raw;
  const int lane = threadIdx.x & 63, i = lane & 15, g = lane >> 4;
  const int wave = __builtin_amdgcn_readfirstlane(threadIdx.x >> 6);
  const int b = blockIdx.x;

  if (wave >= 1 && wave <= NSW) {
    const int s = wave - 1;
    const unsigned base = lds + s * REG;
    {
      const unsigned u = NSW * b + s, yp = (u * 43691u) >> 19;
      const unsigned o1 = u * 3072u, o0 = u * 6144u + yp * 73728u;
      const unsigned v0 = (lane * 16) ^ (lane >= 48 ? 32 : 0), v1 = (lane * 16) ^ (lane < 32 ? 32 : 0), v2 = (lane * 16) ^ (lane >= 16 ? 32 : 0);
      const __amdgpu_buffer_rsrc_t r0 = rsrc(f0, 384u * 384u * 192u), r1 = rsrc(f1, 192u * 192u * 192u);
      unsigned keep, so;
#define G3(R) "buffer_load_dwordx4 %2, " R ", %1 offen nt lds\n\tbuffer_load_dwordx4 %3, " R ", %1 offen offset:1024 nt lds\n\tbuffer_load_dwordx4 %4, " R ", %1 offen offset:2048 nt lds\n\t"
      asm volatile(
          "s_mov_b32 %0, m0\n\t"
          "s_mov_b32 m0, %7\n\ts_mov_b32 %1, %9\n\t" G3("%6")
          "s_add_u32 m0, %7, 3072\n\ts_mov_b32 %1, %8\n\t" G3("%5")
          "s_add_u32 m0, %7, 9216\n\ts_add_u32 %1, %8, 73728\n\t" G3("%5")
          "s_add_u32 m0, %7, 6144\n\ts_add_u32 %1, %8, 3072\n\t" G3("%5")
          "s_add_u32 m0, %7, 12288\n\ts_add_u32 %1, %8, 76800\n\t" G3("%5")
          "s_mov_b32 m0, %0"
          : "=&s"(keep), "=&s"(so)
          : "v"(v0), "v"(v1), "v"(v2), "s"(r0), "s"(r1), "s"(base), "s"(o0), "s"(o1)
          : "memory", "scc");
#undef G3
    }
    const unsigned aoff = base + 3072 + 192 * i + 16 * (g ^ (((i >> 2) & 1) << 1));
    const unsigned foff = base + 192 * (i >> 1) + 16 * (g ^ ((i >> 3) << 1));
    const int tq = (lane >> 2) & 3, tp = lane & 3;
    const unsigned tb = base + 3072 + 192 * (4 * g + tq) + 8 * tp;
    const unsigned tE = tb + 32 * (g & 1), tO = tb - 32 * (g & 1);
    const unsigned fb = base + 192 * (8 * (tq >> 1) + 2 * g + (tq & 1)) + 8 * tp;
    const unsigned fE = fb + 32 * (g >> 1), fO = fb - 32 * (g >> 1);
    const u32x4 ones = {0x3f803f80u, 0x3f803f80u, 0x3f803f80u, 0x3f803f80u};

    BARRIER();
    u32x4 qk[3];
#pragma unroll
    for (int k = 0; k < 3; ++k) qk[k] = lds128(lds + L_QK + 1024 * k + 16 * lane);
    const float sq = *(const LAS float*)(lds + L_SQ + 4 * i);

    f32x4 acc[6];
#pragma unroll
    for (int t = 0; t < 6; ++t) acc[t] = f32x4{0.f, 0.f, 0.f, 0.f};
    float m = 0.f, l = 0.f, p1[4];
#pragma unroll
    for (int h = 0; h < 2; ++h) {
      if (h == 0) WAITVM(9); else WAITVM(3);
      u32x4 F[3];
#pragma unroll
      for (int k = 0; k < 3; ++k) F[k] = lds128(foff + 1536 * h + 64 * k);
      f32x4 d1 = {0.f, 0.f, 0.f, 0.f}, s1f = {0.f, 0.f, 0.f, 0.f};
      float ff = 0.f;
#pragma unroll
      for (int k = 0; k < 3; ++k) {
        d1 = mma32(F[k], qk[k], d1);
        s1f = mma32(F[k], ones, s1f);
#pragma unroll
        for (int e = 0; e < 4; ++e) ff = dot2(F[k][e], F[k][e], ff);
      }
      float lg[2][4], rs[2][4];
#pragma unroll
      for (int dy = 0; dy < 2; ++dy) {
        if (dy == 1) { if (h == 0) WAITVM(6); else WAITVM(0); }
        u32x4 X[3];
#pragma unroll
        for (int k = 0; k < 3; ++k) X[k] = lds128(aoff + 6144 * dy + 3072 * h + 64 * k);
        f32x4 dl = d1, s1 = s1f;
        float xx = ff, xf = 0.f;
#pragma unroll
        for (int k = 0; k < 3; ++k) {
          dl = mma32(X[k], qk[k], dl);
          s1 = mma32(X[k], ones, s1);
#pragma unroll
          for (int e = 0; e < 4; ++e) { xx = dot2(X[k][e], X[k][e], xx); xf = dot2(X[k][e], F[k][e], xf); }
        }
        const float c0 = 96.f * EPS;
        const f32x4 s2 = __builtin_amdgcn_mfma_f32_16x16x4f32(fmaf(2.f, xf, xx), 1.0f, f32x4{c0, c0, c0, c0}, 0, 0, 0);
#pragma unroll
        for (int r = 0; r < 4; ++r) {
          const float mean = s1[r] * (1.f / 96.f);
          const float t = fmaxf(fmaf(-mean, s1[r], s2[r]), c0);
          rs[dy][r] = __builtin_amdgcn_rsqf(t);
          lg[dy][r] = fmaf(-mean, sq, dl[r]) * rs[dy][r];
        }
      }
      float mx = fmaxf(fmaxf(fmaxf(lg[0][0], lg[0][1]), fmaxf(lg[0][2], lg[0][3])), fmaxf(fmaxf(lg[1][0], lg[1][1]), fmaxf(lg[1][2], lg[1][3])));
      mx = xg_max(mx);
      if (h == 0) m = mx;
      else {
        const float mn = fmaxf(m, mx), al = __builtin_amdgcn_exp2f(m - mn);
        m = mn; l *= al; p1[0] *= al; p1[1] *= al;
#pragma unroll
        for (int t = 0; t < 6; ++t) acc[t] *= al;
      }
      float pp[2][4];
#pragma unroll
      for (int dy = 0; dy < 2; ++dy)
#pragma unroll
        for (int r = 0; r < 4; ++r) { const float p = __builtin_amdgcn_exp2f(lg[dy][r] - m); l += p; pp[dy][r] = p * rs[dy][r]; }
      p1[2 * h] = (pp[0][0] + pp[0][1]) + (pp[1][0] + pp[1][1]);
      p1[2 * h + 1] = (pp[0][2] + pp[0][3]) + (pp[1][2] + pp[1][3]);
      const u32x4 P = {pk(pp[0][0], pp[0][1]), pk(pp[0][2], pp[0][3]), pk(pp[1][0], pp[1][1]), pk(pp[1][2], pp[1][3])};
#pragma unroll
      for (int t = 0; t < 6; ++t) {
        const unsigned a = ((t & 1) ? tO : tE) + 3072 * h + 32 * t;
        const u32x2 x0 = ldstr(a), x1 = ldstr(a + 6144);
        acc[t] = mma32(u32x4{x0[0], x0[1], x1[0], x1[1]}, P, acc[t]);
      }
    }
    {
      const u32x2 P1 = {pk(p1[0], p1[1]), pk(p1[2], p1[3])};
#pragma unroll
      for (int t = 0; t < 6; ++t) acc[t] = mma16(ldstr(((t & 1) ? fO : fE) + 32 * t), P1, acc[t]);
    }
    l = xg_sum(l);
#pragma unroll
    for (int t = 0; t < 6; ++t) *(LAS f32x4*)(base + 1024 * t + 16 * lane) = acc[t];
    if (g == 0) { *(LAS float*)(lds + L_ML + 128 * s + 4 * i) = m; *(LAS float*)(lds + L_ML + 128 * s + 64 + 4 * i) = l; }
    BARRIER();
    if (s < 6) {
      float mw[NSW], M = -INFINITY;
#pragma unroll
      for (int w = 0; w < NSW; ++w) { mw[w] = *(const LAS float*)(lds + L_ML + 128 * w + 4 * i); M = fmaxf(M, mw[w]); }
      f32x4 S = {0.f, 0.f, 0.f, 0.f};
#pragma unroll
      for (int w = 0; w < NSW; ++w) S += __builtin_amdgcn_exp2f(mw[w] - M) * *(const LAS f32x4*)(lds + w * REG + 1024 * s + 16 * lane);
      *(LAS u32x2*)(lds + L_QK + 1024 * (s >> 1) + 16 * lane + 8 * (s & 1)) = u32x2{pk(S[0], S[1]), pk(S[2], S[3])};
    }
    BARRIER();
  } else if (wave == 0) {
    const __amdgpu_buffer_rsrc_t rq = rsrc(q, 3072), rwq = rsrc(wq, 18432), rwk = rsrc(wk, 18432);
    u32x4 qf[3];
#pragma unroll
    for (int k = 0; k < 3; ++k) qf[k] = __builtin_amdgcn_raw_buffer_load_b128(rq, 192 * i + 16 * g + 64 * k, 0, 0);
    u32x3 raw[24];
#pragma unroll
    for (int k = 0; k < 24; ++k) raw[k] = __builtin_amdgcn_raw_buffer_load_b96(rwq, 1536 * g + 12 * i + 192 * (k & 7), 6144 * (k >> 3), 0);
    {
      unsigned keep, so; const unsigned v = lane * 16, lb = lds + L_WK;
#define G4 "buffer_load_dwordx4 %2, %3, %1 offen lds\n\tbuffer_load_dwordx4 %2, %3, %1 offen offset:1024 lds\n\tbuffer_load_dwordx4 %2, %3, %1 offen offset:2048 lds\n\t"
#define NX(o) "s_add_u32 m0, %4, " #o "\n\ts_mov_b32 %1, " #o "\n\t"
      asm volatile("s_mov_b32 %0, m0\n\t" NX(0) G4 NX(3072) G4 NX(6144) G4 NX(9216) G4 NX(12288) G4 NX(15360) G4 "s_mov_b32 m0, %0"
                   : "=&s"(keep), "=&s"(so) : "v"(v), "s"(rwk), "s"(lb) : "memory", "scc");
#undef G4
#undef NX
    }
    float x[24], sm = 0.f;
#pragma unroll
    for (int k = 0; k < 12; ++k) { const unsigned w = qf[k >> 2][k & 3]; x[2 * k] = bflo(w); x[2 * k + 1] = bfhi(w); sm += x[2 * k] + x[2 * k + 1]; }
    const float mean = xg_sum(sm) * (1.f / 96.f);
    float vs = 0.f;
#pragma unroll
    for (int k = 0; k < 24; ++k) { x[k] -= mean; vs = fmaf(x[k], x[k], vs); }
    const float rstd = __builtin_amdgcn_rsqf(xg_sum(vs) * (1.f / 96.f) + EPS);
    u32x4 qh[3], ql[3];
#pragma unroll
    for (int k = 0; k < 12; ++k) {
      const float a = x[2 * k] * rstd, c = x[2 * k + 1] * rstd; const unsigned h = pk(a, c);
      qh[k >> 2][k & 3] = h; ql[k >> 2][k & 3] = pk(a - bflo(h), c - bfhi(h));
    }
    f32x4 Q[6];
#pragma unroll
    for (int t = 0; t < 6; ++t) {
      Q[t] = f32x4{0.f, 0.f, 0.f, 0.f};
#pragma unroll
      for (int k = 0; k < 3; ++k) {
        u32x4 A;
#pragma unroll
        for (int e = 0; e < 4; ++e) { const unsigned lo = raw[8 * k + 2 * e][t >> 1], hi = raw[8 * k + 2 * e + 1][t >> 1]; A[e] = __builtin_amdgcn_perm(hi, lo, (t & 1) ? 0x07060302u : 0x05040100u); }
        Q[t] = mma32(A, qh[k], Q[t]);
        Q[t] = mma32(A, ql[k], Q[t]);
      }
    }
    u32x4 Qh[3], Ql[3];
#pragma unroll
    for (int k = 0; k < 12; ++k) {
      const float a = Q[(2 * k) % 6][(2 * k) / 6], c = Q[(2 * k + 1) % 6][(2 * k + 1) / 6]; const unsigned h = pk(a, c);
      Qh[k >> 2][k & 3] = h; Ql[k >> 2][k & 3] = pk(a - bflo(h), c - bfhi(h));
    }
    WAITVM(0);
    const unsigned wa = lds + L_WK + 192 * (8 * (i >> 2) + (i & 3)) + 48 * g;
    float sq = 0.f;
#pragma unroll
    for (int k = 0; k < 3; ++k) {
      f32x4 D[2];
#pragma unroll
      for (int h = 0; h < 2; ++h) {
        D[h] = f32x4{0.f, 0.f, 0.f, 0.f};
#pragma unroll
        for (int j = 0; j < 3; ++j) { const u32x4 A = lds128(wa + 6144 * k + 768 * h + 16 * j); D[h] = mma32(A, Qh[j], D[h]); D[h] = mma32(A, Ql[j], D[h]); }
        D[h] *= LOG2E;
      }
      const u32x4 o = {pk(D[0][0], D[0][1]), pk(D[0][2], D[0][3]), pk(D[1][0], D[1][1]), pk(D[1][2], D[1][3])};
      *(LAS u32x4*)(lds + L_QK + 1024 * k + 16 * lane) = o;
#pragma unroll
      for (int e = 0; e < 4; ++e) sq = dot2(o[e], 0x3f803f80u, sq);
    }
    sq = xg_sum(sq);
    if (g == 0) *(LAS float*)(lds + L_SQ + 4 * i) = sq;
    BARRIER();
    BARRIER();
    BARRIER();
  } else {
    const int hh = wave - (NSW + 1);
    const __amdgpu_buffer_rsrc_t rwv = rsrc(wv, 18432), rwo = rsrc(wo, 18432);
    u32x2 raw[24];
#pragma unroll
    for (int k = 0; k < 24; ++k) raw[k] = __builtin_amdgcn_raw_buffer_load_b64(rwo, 1536 * g + 12 * i + 192 * (k & 7), 6144 * (k >> 3) + 4 * hh, 0);
    u32x4 A[18];
#pragma unroll
    for (int k = 0; k < 18; ++k) A[k] = __builtin_amdgcn_raw_buffer_load_b128(rwv, 192 * i + 16 * g + 64 * (k % 3), 3072 * (k / 3), 0);
    u32x4 B[3][3];
#pragma unroll
    for (int k = 0; k < 12; ++k) {
      const unsigned l0 = raw[2 * k][0], l1 = raw[2 * k][1], h0 = raw[2 * k + 1][0], h1 = raw[2 * k + 1][1];
      const unsigned sa = hh ? 0x07060302u : 0x05040100u, sb = hh ? 0x05040100u : 0x07060302u;
      B[0][k >> 2][k & 3] = __builtin_amdgcn_perm(h0, l0, sa);
      B[1][k >> 2][k & 3] = __builtin_amdgcn_perm(hh ? h1 : h0, hh ? l1 : l0, sb);
      B[2][k >> 2][k & 3] = __builtin_amdgcn_perm(h1, l1, sa);
    }
    f32x4 D[3][6];
#pragma unroll
    for (int t = 0; t < 6; ++t)
#pragma unroll
      for (int qq = 0; qq < 3; ++qq) {
        D[qq][t] = f32x4{0.f, 0.f, 0.f, 0.f};
#pragma unroll
        for (int k = 0; k < 3; ++k) D[qq][t] = mma32(A[3 * t + k], B[qq][k], D[qq][t]);
      }
    u32x4 W[3][3];
#pragma unroll
    for (int qq = 0; qq < 3; ++qq) {
      float cs = 0.f;
#pragma unroll
      for (int t = 0; t < 6; ++t) cs += (D[qq][t][0] + D[qq][t][1]) + (D[qq][t][2] + D[qq][t][3]);
      const float sc = 9.797958971132712f, mu = xg_sum(cs) * (sc / 96.f);
#pragma unroll
      for (int t = 0; t < 6; ++t) {
        W[qq][t >> 1][2 * (t & 1)] = pk(fmaf(D[qq][t][0], sc, -mu), fmaf(D[qq][t][1], sc, -mu));
        W[qq][t >> 1][2 * (t & 1) + 1] = pk(fmaf(D[qq][t][2], sc, -mu), fmaf(D[qq][t][3], sc, -mu));
      }
    }
    BARRIER();
    BARRIER();
    float mw[NSW], M = -INFINITY, L = 0.f;
#pragma unroll
    for (int w = 0; w < NSW; ++w) { mw[w] = *(const LAS float*)(lds + L_ML + 128 * w + 4 * i); M = fmaxf(M, mw[w]); }
#pragma unroll
    for (int w = 0; w < NSW; ++w) L = fmaf(__builtin_amdgcn_exp2f(mw[w] - M), *(const LAS float*)(lds + L_ML + 128 * w + 64 + 4 * i), L);
    BARRIER();
    u32x4 S[3];
#pragma unroll
    for (int k = 0; k < 3; ++k) S[k] = lds128(lds + L_QK + 1024 * k + 16 * lane);
    float* po = part + (size_t)(b * 16 + i) * 100;
#pragma unroll
    for (int qq = 0; qq < 3; ++qq) {
      f32x4 T = {0.f, 0.f, 0.f, 0.f};
#pragma unroll
      for (int k = 0; k < 3; ++k) T = mma32(W[qq][k], S[k], T);
#pragma unroll
      for (int r = 0; r < 4; ++r) po[24 * g + 6 * r + 3 * hh + qq] = T[r];
    }
    if (g == 0 && hh == 0) { po[96] = M; po[97] = L; }
  }
}

__global__ void __launch_bounds__(768) k_fin(const u16* __restrict__ q, const float* __restrict__ part, u16* __restrict__ out) {
  __shared__ float sT[8][96], sM[8], sL[8];
  const int n = blockIdx.x, e = threadIdx.x % 96, gr = threadIdx.x / 96;
  const float* p = part + (size_t)(gr * 32 * 16 + n) * 100;
  float mv[32], lv[32], tv[32];
#pragma unroll
  for (int k = 0; k < 32; ++k) { mv[k] = p[k * 1600 + 96]; lv[k] = p[k * 1600 + 97]; tv[k] = p[k * 1600 + e]; }
  float M = -INFINITY;
#pragma unroll
  for (int k = 0; k < 32; ++k) M = fmaxf(M, mv[k]);
  float L = 0.f, T = 0.f;
#pragma unroll
  for (int k = 0; k < 32; ++k) { const float w = __builtin_amdgcn_exp2f(mv[k] - M); L = fmaf(w, lv[k], L); T = fmaf(w, tv[k], T); }
  sT[gr][e] = T; if (e == 0) { sM[gr] = M; sL[gr] = L; }
  __syncthreads();
  if (gr == 0) {
    float MM = -INFINITY;
#pragma unroll
    for (int k = 0; k < 8; ++k) MM = fmaxf(MM, sM[k]);
    float LL = 0.f, TT = 0.f;
#pragma unroll
    for (int k = 0; k < 8; ++k) { const float w = __builtin_amdgcn_exp2f(sM[k] - MM); LL = fmaf(w, sL[k], LL); TT = fmaf(w, sT[k][e], TT); }
    const float o = __uint_as_float((unsigned)q[n * 96 + e] << 16) + TT / LL;
    out[n * 96 + e] = (u16)(pk(o, o) & 0xffff);
  }
}


#define GAS __attribute__((address_space(1)))
DEVI unsigned long long rt() { return __builtin_amdgcn_s_memrealtime(); }
__global__ void __launch_bounds__(576) k_diag(const char* f0, const char* f1, unsigned long long* ts) {
  __shared__ __attribute__((aligned(16))) char dl[138240];
  unsigned long long t0 = rt();
  const int wave = __builtin_amdgcn_readfirstlane(threadIdx.x >> 6), lane = threadIdx.x & 63, b = blockIdx.x;
  const char* p0 = f0 + (size_t)b * 110592 + wave * 12288 + lane * 16; const char* p1 = f1 + (size_t)b * 27648 + wave * 3072 + lane * 16;
#pragma unroll
  for (int i = 0; i < 15; i++) {
    const char* a = i < 3 ? p1 + i * 1024 : p0 + (i - 3) * 1024;
    __builtin_amdgcn_global_load_lds((const GAS unsigned*)a, (LAS unsigned*)(dl + (wave * 15 + i) * 1024), 16, 0, AUX);
  }
  asm volatile("s_waitcnt vmcnt(14)" ::: "memory");
  unsigned long long t2 = rt();
  asm volatile("s_waitcnt vmcnt(0)" ::: "memory");
  unsigned acc = *(unsigned*)(dl + wave * 1024 + lane * 4);
  unsigned long long t1 = rt();
  if (acc == 0x12345678) t1++;
  if (lane == 0) { ts[(b * 16 + wave) * 4] = t0; ts[(b * 16 + wave) * 4 + 1] = t1; ts[(b * 16 + wave) * 4 + 2] = t2; }
}
__global__ void __launch_bounds__(256) k_spin(const unsigned long long* ts) {
  __shared__ unsigned long long r[6][256];
  const int b = threadIdx.x;
  unsigned long long mn0 = ~0ull, mx0 = 0, e = 0, s2 = 0;
#pragma unroll
  for (int w = 0; w < 9; ++w) { const unsigned long long* t = ts + (b * 16 + w) * 4; mn0 = min(mn0, t[0]); mx0 = max(mx0, t[0]); e = max(e, t[1]); if (w == 0) s2 = t[2] - t[0]; }
  r[0][b] = mn0; r[1][b] = mx0; r[2][b] = e; r[3][b] = e; r[4][b] = e; r[5][b] = s2;
  __syncthreads();
  for (int o = 128; o > 0; o >>= 1) { if (b < o) { r[0][b] = min(r[0][b], r[0][b + o]); r[1][b] = max(r[1][b], r[1][b + o]); r[2][b] = max(r[2][b], r[2][b + o]); r[3][b] = min(r[3][b], r[3][b + o]); r[4][b] += r[4][b + o]; r[5][b] += r[5][b + o]; } __syncthreads(); }
  unsigned long long v[6] = {r[2][0] - r[0][0], r[1][0] - r[0][0], r[5][0] / 256, r[3][0] - r[0][0], r[4][0] / 256 - r[0][0], 0};
#pragma unroll
  for (int k = 0; k < 6; ++k) asm volatile("" : "+v"(v[k]));
  const unsigned long long until = rt() + 20 * v[STAT];
  while (rt() < until) ;
}
extern "C" void kernel_launch(void* const* d_in, const int* in_sizes, int n_in, void* d_out, int out_size, void* d_ws, size_t ws_size,
                              hipStream_t stream) {
  const u16* q = (const u16*)d_in[0]; const u16* f0 = (const u16*)d_in[1]; const u16* f1 = (const u16*)d_in[2];
  const u16* wq = (const u16*)d_in[3]; const u16* wk = (const u16*)d_in[4]; const u16* wv = (const u16*)d_in[5]; const u16* wo = (const u16*)d_in[6];
  float* part = (float*)d_ws;
  unsigned long long* ts = (unsigned long long*)((char*)d_ws + (64u << 20));
  k_diag<<<256, 576, 0, stream>>>((const char*)f0, (const char*)f1, ts);
  k_main<<<NWG, 768, 0, stream>>>(q, f0, f1, wq, wk, wv, wo, part);
  k_fin<<<16, 768, 0, stream>>>(q, part, (u16*)d_out);
  k_spin<<<1, 256, 0, stream>>>(ts);
}
```

```cpp
#include <hip/hip_runtime.h>
#include <hip/hip_bf16.h>
#pragma clang diagnostic ignored "-Wint-to-pointer-cast"

typedef unsigned short u16;
typedef __attribute__((ext_vector_type(4))) short s16x4;
typedef __attribute__((ext_vector_type(8))) __bf16 bf16x8;
typedef __attribute__((ext_vector_type(2))) __bf16 bf16x2;
typedef __attribute__((ext_vector_type(2))) float f32x2;
typedef __attribute__((ext_vector_type(4))) float f32x4;
typedef __attribute__((ext_vector_type(4))) unsigned u32x4;
typedef __attribute__((ext_vector_type(2))) unsigned u32x2;
#define LAS __attribute__((address_space(3)))
#define GAS __attribute__((address_space(1)))
#define DEVI __device__ __forceinline__

#define PREWRITE 1
#define DRAIN 1
#define WPOL ""
#define WAUX 0
#define VSTART 4
#define CODEPF 8192
constexpr int NWG = 256;
constexpr int NH = 3;
constexpr int NSW = 9;
constexpr int NCONS = 96;
constexpr int REG = 15360;
constexpr int NLATE = 3;
constexpr int L_WQ = (NSW - NLATE) * REG;
constexpr int L_WK = L_WQ + 18432;
constexpr int L_Q = L_WK + 18432;
constexpr int L_WA = NSW * REG;
constexpr int L_QK = L_WA + 18432;
constexpr int L_M = L_QK + 3072;
constexpr int L_L = L_M + 768;
constexpr int L_FL = L_L + 768;
constexpr int L_END = L_FL + 32;
constexpr unsigned INBOX = 16384 + 256;
constexpr unsigned WS_EPOCH = NCONS * INBOX;
constexpr float EPS = 1e-5f;
constexpr float LOG2E = 1.4426950408889634f;
constexpr float THR = 40.f;

DEVI float bflo(unsigned x) { return __uint_as_float(x << 16); }
DEVI float bfhi(unsigned x) { return __uint_as_float(x & 0xffff0000u); }
DEVI unsigned pk(float a, float b) { f32x2 p = {a, b}; bf16x2 h = __builtin_convertvector(p, bf16x2); return __builtin_bit_cast(unsigned, h); }
DEVI f32x4 mma32(u32x4 a, u32x4 b, f32x4 c) { return __builtin_amdgcn_mfma_f32_16x16x32_bf16(__builtin_bit_cast(bf16x8, a), __builtin_bit_cast(bf16x8, b), c, 0, 0, 0); }
DEVI f32x4 mma4(float a, float b, f32x4 c) { return __builtin_amdgcn_mfma_f32_16x16x4f32(a, b, c, 0, 0, 0); }
DEVI float xg_sum(float x) {
  auto s = __builtin_amdgcn_permlane16_swap(__float_as_uint(x), __float_as_uint(x), false, false);
  x = __uint_as_float(s[0]) + __uint_as_float(s[1]);
  auto t = __builtin_amdgcn_permlane32_swap(__float_as_uint(x), __float_as_uint(x), false, false);
  return __uint_as_float(t[0]) + __uint_as_float(t[1]);
}
DEVI float xg_max(float x) {
  auto s = __builtin_amdgcn_permlane16_swap(__float_as_uint(x), __float_as_uint(x), false, false);
  x = fmaxf(__uint_as_float(s[0]), __uint_as_float(s[1]));
  auto t = __builtin_amdgcn_permlane32_swap(__float_as_uint(x), __float_as_uint(x), false, false);
  return fmaxf(__uint_as_float(t[0]), __uint_as_float(t[1]));
}
template <int CTRL> DEVI float dpp(float x) { return __builtin_bit_cast(float, __builtin_amdgcn_mov_dpp(__builtin_bit_cast(int, x), CTRL, 0xf, 0xf, true)); }
constexpr int ROR4 = 0x124, ROR8 = 0x128;
DEVI u32x4 lds128(unsigned a) { return *(const LAS u32x4*)a; }
DEVI f32x4 ldsf4(unsigned a) { return *(const LAS f32x4*)a; }
DEVI u32x2 ldstr(unsigned a) { s16x4 t = __builtin_amdgcn_ds_read_tr16_b64_v4i16((LAS s16x4*)a); return __builtin_bit_cast(u32x2, t); }
DEVI u32x4 ldstr2(unsigned a, unsigned c) { const u32x2 x = ldstr(a), y = ldstr(c); return u32x4{x[0], x[1], y[0], y[1]}; }
DEVI __amdgpu_buffer_rsrc_t rsrc(const void* p, unsigned bytes) { return __builtin_amdgcn_make_buffer_rsrc((void*)p, 0, bytes, 0x00020000); }
#define BARRIER() do { asm volatile("s_waitcnt lgkmcnt(0)" ::: "memory"); __builtin_amdgcn_s_barrier(); asm volatile("" ::: "memory"); } while (0)
#define WAITVM(n) asm volatile("s_waitcnt vmcnt(" #n ")" ::: "memory")
#define PIN(x) asm volatile("" : "+v"(x))
DEVI void post(unsigned a, int lane) {
  asm volatile("s_waitcnt lgkmcnt(0)" ::: "memory");
  if (lane == 0) __hip_atomic_fetch_add((LAS unsigned*)a, 1u, __ATOMIC_RELAXED, __HIP_MEMORY_SCOPE_WORKGROUP);
}
DEVI void await(unsigned a, unsigned n) {
  while (__builtin_amdgcn_readfirstlane(__hip_atomic_load((LAS unsigned*)a, __ATOMIC_RELAXED, __HIP_MEMORY_SCOPE_WORKGROUP)) < n) __builtin_amdgcn_s_sleep(1);
  asm volatile("" ::: "memory");
}
#define DMA3(POL, r, so, la, va, vb, vc) do { unsigned keep_; \
  asm volatile("s_mov_b32 %0, m0\n\ts_mov_b32 m0, %6\n\ts_nop 0\n\t" \
               "buffer_load_dwordx4 %1, %4, %5 offen " POL "lds\n\tbuffer_load_dwordx4 %2, %4, %5 offen offset:1024 " POL "lds\n\t" \
               "buffer_load_dwordx4 %3, %4, %5 offen offset:2048 " POL "lds\n\ts_mov_b32 m0, %0" \
               : "=&s"(keep_) : "v"(va), "v"(vb), "v"(vc), "s"(r), "s"(so), "s"(la) : "memory"); } while (0)

#define STAMP(k)
#define STAMP_DECL
#define STAMP_OUT()

__global__ void __launch_bounds__(768) k_main(const u16* __restrict__ q, const u16* __restrict__ f0, const u16* __restrict__ f1,
                                              const u16* __restrict__ wq, const u16* __restrict__ wk, const u16* __restrict__ wv,
                                              const u16* __restrict__ wo, u16* out, char* ws) {
  __shared__ __attribute__((aligned(16))) char lds_raw[L_END];
  const unsigned lds = (unsigned)(size_t)(LAS char*)lds_raw;
  const int lane = threadIdx.x & 63, i = lane & 15, g = lane >> 4;
  const int tq = (lane >> 2) & 3, tp = lane & 3;
  const int wave = __builtin_amdgcn_readfirstlane(threadIdx.x >> 6);
  const int b = blockIdx.x;
  const f32x4 z4 = {0.f, 0.f, 0.f, 0.f};
  STAMP_DECL; STAMP(0);
  if (threadIdx.x < 4) *(LAS unsigned*)(lds + L_FL + 4 * threadIdx.x) = 0u;
  BARRIER();
  const __amdgpu_buffer_rsrc_t rws = rsrc(ws, WS_EPOCH + 64);
  u32x2 qres;

  if (wave >= NH) {
    const int s = wave - NH;
    const unsigned base = lds + s * REG;
    const unsigned u = NSW * b + s, yp = (u * 43691u) >> 19;
    const unsigned o0 = u * 6144u + yp * 73728u;
    const unsigned v0 = (lane * 16) ^ (lane >= 48 ? 32 : 0), v1 = (lane * 16) ^ (lane < 32 ? 32 : 0), v2 = (lane * 16) ^ (lane >= 16 ? 32 : 0);
    const __amdgpu_buffer_rsrc_t r0 = rsrc(f0, 384u * 384u * 192u), r1 = rsrc(f1, 192u * 192u * 192u);
    if (s >= NSW - NLATE) await(lds + L_FL + 8, 1);
    DMA3("nt ", r1, u * 3072u, base, v0, v1, v2);
    DMA3("nt ", r0, o0, base + 3072, v0, v1, v2);
    DMA3("nt ", r0, o0 + 73728u, base + 6144, v0, v1, v2);
    DMA3("nt ", r0, o0 + 3072u, base + 9216, v0, v1, v2);
    DMA3("nt ", r0, o0 + 76800u, base + 12288, v0, v1, v2);
    const unsigned aoff = base + 192 * i + 16 * (g ^ (((i >> 2) & 1) << 1));
    const unsigned foff = base + 192 * (i >> 1) + 16 * (g ^ ((i >> 3) << 1));
    const unsigned tb = base + 192 * (4 * g + tq) + 8 * tp;
    const unsigned tE = tb + 32 * (g & 1), tO = tb - 32 * (g & 1);
    const unsigned fb = base + 192 * (2 * g + (tq >> 1)) + 8 * tp;
    const unsigned fE = fb + 32 * (g >> 1), fO = fb - 32 * (g >> 1);
    u32x4 ones = {0x3f803f80u, 0x3f803f80u, 0x3f803f80u, 0x3f803f80u};
    PIN(ones);
    const bool dg = g == (i >> 2);
    const float dmask = dg ? 1.f : 0.f, kneg = dg ? -1.f / 96.f : 0.f;
    const bool b0 = i & 1, b1 = i & 2;
    const float c0 = 96.f * EPS;
    STAMP(1);
    await(lds + L_FL + 8, 1);
    STAMP(2);
    u32x4 qk[3];
#pragma unroll
    for (int k = 0; k < 3; ++k) qk[k] = lds128(lds + L_QK + 1024 * k + 16 * lane);
    f32x4 acc[6];
#pragma unroll
    for (int t = 0; t < 6; ++t) acc[t] = z4;
    float m = 0.f, l = 0.f;
#pragma unroll
    for (int h = 0; h < 2; ++h) {
      if (h == 0) WAITVM(9); else WAITVM(3);
      if (h == 0) STAMP(3);
      u32x4 F[3], At[6];
#pragma unroll
      for (int k = 0; k < 3; ++k) F[k] = lds128(foff + 1536 * h + 64 * k);
#pragma unroll
      for (int t = 0; t < 6; ++t) { const u32x2 y = ldstr(((t & 1) ? fO : fE) + 1536 * h + 32 * t); At[t][2] = y[0]; At[t][3] = y[1]; }
      f32x4 d1 = z4, s1f = z4, gff = {c0, c0, c0, c0};
#pragma unroll
      for (int dy = 0; dy < 2; ++dy) {
        if (dy == 1) { if (h == 0) WAITVM(6); else WAITVM(0); }
        if (dy == 1 && h == 1) STAMP(4);
        const int slot = 3072 * (1 + dy + 2 * h);
        u32x4 X[3];
#pragma unroll
        for (int k = 0; k < 3; ++k) X[k] = lds128(aoff + slot + 64 * k);
#pragma unroll
        for (int t = 0; t < 6; ++t) { const u32x2 y = ldstr(((t & 1) ? tO : tE) + slot + 32 * t); At[t][0] = y[0]; At[t][1] = y[1]; }
        if (dy == 0) {
#pragma unroll
          for (int k = 0; k < 3; ++k) { gff = mma32(F[k], F[k], gff); s1f = mma32(ones, F[k], s1f); d1 = mma32(F[k], qk[k], d1); }
        }
        f32x4 gxx = gff, gxf = z4, s1 = s1f, dl = d1;
#pragma unroll
        for (int k = 0; k < 3; ++k) { gxx = mma32(X[k], X[k], gxx); gxf = mma32(X[k], F[k], gxf); s1 = mma32(ones, X[k], s1); }
#pragma unroll
        for (int k = 0; k < 3; ++k) dl = mma32(X[k], qk[k], dl);
        const float u0 = fmaf(2.f, gxf[0], gxx[0]), u1 = fmaf(2.f, gxf[1], gxx[1]), u2 = fmaf(2.f, gxf[2], gxx[2]), u3 = fmaf(2.f, gxf[3], gxx[3]);
        const float ua = b0 ? u1 : u0, ub = b0 ? u3 : u2, ss = b1 ? ub : ua;
        const float rsm = __builtin_amdgcn_rsqf(fmaxf(fmaf(s1[0] * kneg, s1[0], ss), c0)) * dmask;
        const f32x4 rs = mma4(rsm, 1.f, z4);
        float a[4];
        if (h == 0 && dy == 0) {
#pragma unroll
          for (int r = 0; r < 4; ++r) a[r] = dl[r] * rs[r];
          m = xg_max(fmaxf(fmaxf(a[0], a[1]), fmaxf(a[2], a[3])));
#pragma unroll
          for (int r = 0; r < 4; ++r) a[r] -= m;
        } else {
#pragma unroll
          for (int r = 0; r < 4; ++r) a[r] = fmaf(dl[r], rs[r], -m);
          const float mx = fmaxf(fmaxf(a[0], a[1]), fmaxf(a[2], a[3]));
          if (__builtin_expect(__any(mx > THR), 0)) {
            const float d = fmaxf(xg_max(mx), 0.f), al = __builtin_amdgcn_exp2f(-d);
            m += d; l *= al;
#pragma unroll
            for (int t = 0; t < 6; ++t) acc[t] *= al;
#pragma unroll
            for (int r = 0; r < 4; ++r) a[r] -= d;
          }
        }
        float pp[4];
#pragma unroll
        for (int r = 0; r < 4; ++r) { const float p = __builtin_amdgcn_exp2f(a[r]); l += p; pp[r] = p * rs[r]; }
        const unsigned pa = pk(pp[0], pp[1]), pb = pk(pp[2], pp[3]);
        const u32x4 P = {pa, pb, pa, pb};
#pragma unroll
        for (int t = 0; t < 6; ++t) acc[t] = mma32(At[t], P, acc[t]);
        if (!(h == 1 && dy == 1)) {
#pragma unroll
          for (int t = 0; t < 6; ++t) PIN(acc[t]);
          PIN(l);
        }
      }
    }
    l = xg_sum(l);
#pragma unroll
    for (int t = 0; t < 6; ++t) *(LAS f32x4*)(base + 1024 * t + 16 * lane) = acc[t];
    if (g == 0) { *(LAS float*)(lds + L_M + 48 * i + 4 * s) = m; *(LAS float*)(lds + L_L + 48 * i + 4 * s) = l; }
    STAMP(5);
    BARRIER();
    STAMP(6);
    if (s < 6) {
      const f32x4 m0 = ldsf4(lds + L_M + 48 * i), m1 = ldsf4(lds + L_M + 48 * i + 16);
      const float m8 = *(const LAS float*)(lds + L_M + 48 * i + 32);
      f32x4 pw[NSW];
#pragma unroll
      for (int w = 0; w < NSW; ++w) pw[w] = ldsf4(lds + w * REG + 1024 * s + 16 * lane);
      const float M = fmaxf(fmaxf(fmaxf(fmaxf(m0[0], m0[1]), m0[2]), fmaxf(fmaxf(m0[3], m1[0]), m1[1])), fmaxf(fmaxf(m1[2], m1[3]), m8));
      f32x4 S = __builtin_amdgcn_exp2f(m8 - M) * pw[8];
#pragma unroll
      for (int w = 0; w < 4; ++w) { S += __builtin_amdgcn_exp2f(m0[w] - M) * pw[w]; S += __builtin_amdgcn_exp2f(m1[w] - M) * pw[4 + w]; }
      *(LAS u32x2*)(lds + L_QK + 1024 * (s >> 1) + 16 * lane + 8 * (s & 1)) = u32x2{pk(S[0], S[1]), pk(S[2], S[3])};
    }
    BARRIER();
    STAMP(7);
  } else {
    const int k = wave;
    const unsigned vl = lane * 16;
    if (k < 2) {
      const __amdgpu_buffer_rsrc_t rq = rsrc(q, 3072), rwq = rsrc(wq, 18432), rwk = rsrc(wk, 18432);
      const unsigned so = 9216 * k;
      if (k == 0) DMA3(WPOL, rq, 0, lds + L_Q, vl, vl, vl);
#pragma unroll
      for (int j = 0; j < 3; ++j) DMA3(WPOL, rwq, so + 3072 * j, lds + L_WQ + so + 3072 * j, vl, vl, vl);
#pragma unroll
      for (int j = 0; j < 3; ++j) DMA3(WPOL, rwk, so + 3072 * j, lds + L_WK + so + 3072 * j, vl, vl, vl);
      STAMP(1);
      if (k == 1) {
        WAITVM(9); post(lds + L_FL, lane);
        WAITVM(0); post(lds + L_FL + 4, lane);
      } else {
        WAITVM(18);
        u32x4 qn[3];
        {
          float x[24], sm = 0.f;
#pragma unroll
          for (int s = 0; s < 3; ++s) {
            const u32x4 w4 = lds128(lds + L_Q + 192 * i + 64 * s + 16 * g);
#pragma unroll
            for (int j = 0; j < 4; ++j) { const unsigned w = w4[j]; x[8 * s + 2 * j] = bflo(w); x[8 * s + 2 * j + 1] = bfhi(w); sm += bflo(w) + bfhi(w); }
          }
          const float mean = xg_sum(sm) * (1.f / 96.f);
          float vs = 0.f;
#pragma unroll
          for (int j = 0; j < 24; ++j) { x[j] -= mean; vs = fmaf(x[j], x[j], vs); }
          const float rstd = __builtin_amdgcn_rsqf(xg_sum(vs) * (1.f / 96.f) + EPS);
#pragma unroll
          for (int j = 0; j < 12; ++j) qn[j >> 2][j & 3] = pk(x[2 * j] * rstd, x[2 * j + 1] * rstd);
#pragma unroll
          for (int s = 0; s < 3; ++s) PIN(qn[s]);
        }
        WAITVM(9);
        await(lds + L_FL, 1);
        STAMP(2);
        u32x4 Qb[3];
        const unsigned ta = lds + L_WQ + 192 * (8 * g + tq) + 16 * tp;
#pragma unroll
        for (int kk = 0; kk < 3; ++kk) {
          f32x4 T[2] = {z4, z4};
#pragma unroll
          for (int s = 0; s < 3; ++s)
#pragma unroll
            for (int h = 0; h < 2; ++h) T[h] = mma32(ldstr2(ta + 6144 * s + 64 * kk + 8 * h, ta + 6144 * s + 768 + 64 * kk + 8 * h), qn[s], T[h]);
          Qb[kk] = u32x4{pk(T[0][0], T[0][1]), pk(T[0][2], T[0][3]), pk(T[1][0], T[1][1]), pk(T[1][2], T[1][3])};
          PIN(Qb[kk]);
        }
        WAITVM(0);
        await(lds + L_FL + 4, 1);
        STAMP(3);
        const unsigned wa = lds + L_WK + 192 * (8 * (i >> 2) + (i & 3)) + 16 * g;
        f32x4 D[6]; float cs = 0.f;
#pragma unroll
        for (int t = 0; t < 6; ++t) {
          D[t] = z4;
#pragma unroll
          for (int kk = 0; kk < 3; ++kk) D[t] = mma32(lds128(wa + 6144 * (t >> 1) + 768 * (t & 1) + 64 * kk), Qb[kk], D[t]);
          cs += (D[t][0] + D[t][1]) + (D[t][2] + D[t][3]);
        }
        const float mu = xg_sum(cs) * (LOG2E / 96.f);
#pragma unroll
        for (int s = 0; s < 3; ++s)
          *(LAS u32x4*)(lds + L_QK + 1024 * s + 16 * lane) =
              u32x4{pk(fmaf(D[2 * s][0], LOG2E, -mu), fmaf(D[2 * s][1], LOG2E, -mu)), pk(fmaf(D[2 * s][2], LOG2E, -mu), fmaf(D[2 * s][3], LOG2E, -mu)),
                    pk(fmaf(D[2 * s + 1][0], LOG2E, -mu), fmaf(D[2 * s + 1][1], LOG2E, -mu)), pk(fmaf(D[2 * s + 1][2], LOG2E, -mu), fmaf(D[2 * s + 1][3], LOG2E, -mu))};
        post(lds + L_FL + 8, lane);
      }
    } else {
      unsigned pf = 0;
      if (CODEPF && lane < 4) {
        const unsigned off = (b >> 3) * 512 + lane * 128;
        if (off < CODEPF) pf = *(const GAS unsigned*)((__builtin_amdgcn_s_getpc() & ~0xffull) + off);
      }
      unsigned epoch = __builtin_amdgcn_raw_buffer_load_b32(rws, 0, WS_EPOCH, 16);
      const int c = b < NCONS ? b : 0;
      qres = *(const GAS u32x2*)(q + (4 * (c / 24) + tp) * 96 + 4 * (c % 24));
      PIN(epoch); PIN(qres); PIN(pf);
      if (lane == 0) *(LAS unsigned*)(lds + L_FL + 16) = epoch + 1u;
    }
    if (k == 2) await(lds + L_FL + VSTART, 1);
    STAMP(4);
    u32x4 Bt[2][3];
    {
      const __amdgpu_buffer_rsrc_t rwv = rsrc(wv, 18432), rwo = rsrc(wo, 18432);
      const unsigned so = 6144 * k;
#pragma unroll
      for (int j = 0; j < 2; ++j) DMA3(WPOL, rwv, so + 3072 * j, lds + L_WA + so + 3072 * j, vl, vl, vl);
      const unsigned rowo = 192 * (8 * (i >> 2) + (i & 3)) + 16 * g;
      u32x4 w[6];
#pragma unroll
      for (int j = 0; j < 6; ++j) w[j] = __builtin_amdgcn_raw_buffer_load_b128(rwo, rowo + 768 * (j & 1), 6144 * (j >> 1) + 64 * k, WAUX);
      const unsigned one = (i & 1) ? 0x3f800000u : 0x00003f80u;
#pragma unroll
      for (int h = 0; h < 2; ++h) {
        u32x4 id;
#pragma unroll
        for (int e = 0; e < 4; ++e) id[e] = (g == 2 * h + (i >> 3) && ((i & 7) >> 1) == e) ? one : 0u;
#pragma unroll
        for (int s = 0; s < 3; ++s) {
          const f32x4 a = mma32(w[2 * s], id, z4), c = mma32(w[2 * s + 1], id, z4);
          Bt[h][s] = u32x4{pk(a[0], a[1]), pk(a[2], a[3]), pk(c[0], c[1]), pk(c[2], c[3])};
          PIN(Bt[h][s]);
        }
      }
    }
    WAITVM(0);
    post(lds + L_FL + 12, lane);
    await(lds + L_FL + 12, NH);
    const unsigned tag = *(const LAS unsigned*)(lds + L_FL + 16);
    const unsigned oaddr = (((i >> 2) * 3 + k) * 8 + g) * INBOX + b * 64 + (i & 3) * 16;
    if (PREWRITE) {
      __builtin_amdgcn_raw_buffer_store_b128(u32x4{0u, 0u, 0u, tag - 1u}, rws, oaddr, 0, 16);
      __builtin_amdgcn_raw_buffer_store_b128(u32x4{0u, 0u, 0u, tag - 1u}, rws, oaddr, 4 * INBOX, 16);
    }
    u32x4 W[2][3];
    {
      f32x4 D[2][6];
#pragma unroll
      for (int t = 0; t < 6; ++t) {
        D[0][t] = z4; D[1][t] = z4;
#pragma unroll
        for (int s = 0; s < 3; ++s) {
          const u32x4 A = lds128(lds + L_WA + 3072 * t + 192 * i + 64 * s + 16 * g);
          D[0][t] = mma32(A, Bt[0][s], D[0][t]); D[1][t] = mma32(A, Bt[1][s], D[1][t]);
        }
      }
#pragma unroll
      for (int h = 0; h < 2; ++h) {
        float cs = 0.f;
#pragma unroll
        for (int t = 0; t < 6; ++t) cs += (D[h][t][0] + D[h][t][1]) + (D[h][t][2] + D[h][t][3]);
        const float sc = 9.797958971132712f, mu = xg_sum(cs) * (sc / 96.f);
#pragma unroll
        for (int t = 0; t < 6; ++t) {
          W[h][t >> 1][2 * (t & 1)] = pk(fmaf(D[h][t][0], sc, -mu), fmaf(D[h][t][1], sc, -mu));
          W[h][t >> 1][2 * (t & 1) + 1] = pk(fmaf(D[h][t][2], sc, -mu), fmaf(D[h][t][3], sc, -mu));
        }
#pragma unroll
        for (int s = 0; s < 3; ++s) PIN(W[h][s]);
      }
    }
    WAITVM(0);
    STAMP(5);
    BARRIER();
    float lse, inv;
    {
      const f32x4 m0 = ldsf4(lds + L_M + 48 * i), m1 = ldsf4(lds + L_M + 48 * i + 16), l0 = ldsf4(lds + L_L + 48 * i), l1 = ldsf4(lds + L_L + 48 * i + 16);
      const float m8 = *(const LAS float*)(lds + L_M + 48 * i + 32), l8 = *(const LAS float*)(lds + L_L + 48 * i + 32);
      const float M = fmaxf(fmaxf(fmaxf(fmaxf(m0[0], m0[1]), m0[2]), fmaxf(fmaxf(m0[3], m1[0]), m1[1])), fmaxf(fmaxf(m1[2], m1[3]), m8));
      float L = __builtin_amdgcn_exp2f(m8 - M) * l8;
#pragma unroll
      for (int w = 0; w < 4; ++w) { L = fmaf(__builtin_amdgcn_exp2f(m0[w] - M), l0[w], L); L = fmaf(__builtin_amdgcn_exp2f(m1[w] - M), l1[w], L); }
      lse = M + __builtin_amdgcn_logf(L); inv = __builtin_amdgcn_rcpf(L);
      PIN(lse); PIN(inv);
    }
    BARRIER();
    STAMP(6);
    u32x4 S[3];
#pragma unroll
    for (int s = 0; s < 3; ++s) S[s] = lds128(lds + L_QK + 1024 * s + 16 * lane);
#pragma unroll
    for (int h = 0; h < 2; ++h) {
      f32x4 T = z4;
#pragma unroll
      for (int s = 0; s < 3; ++s) T = mma32(W[h][s], S[s], T);
      const u32x4 gr = {pk(T[0] * inv, T[1] * inv), pk(T[2] * inv, T[3] * inv), __float_as_uint(lse), tag};
      __builtin_amdgcn_raw_buffer_store_b128(gr, rws, oaddr, 4 * INBOX * h, 16);
    }
    STAMP(7);
    if (DRAIN && b < NCONS) WAITVM(0);
  }
  if (b < NCONS) {
    const unsigned tag = *(const LAS unsigned*)(lds + L_FL + 16);
    const unsigned ia = b * INBOX + wave * 1024 + lane * 16;
    BARRIER();
    STAMP(8);
    if (wave < 8) {
      u32x4 g0 = __builtin_amdgcn_raw_buffer_load_b128(rws, ia, 0, 16), g1 = __builtin_amdgcn_raw_buffer_load_b128(rws, ia, 8192, 16);
      for (unsigned spin = 0; spin < (1u << 20); ++spin) {
        const bool m0 = g0[3] != tag, m1 = g1[3] != tag;
        if (!__any(m0 | m1)) break;
        asm volatile("" ::: "memory");
        if (m0) g0 = __builtin_amdgcn_raw_buffer_load_b128(rws, ia, 0, 16);
        if (m1) g1 = __builtin_amdgcn_raw_buffer_load_b128(rws, ia, 8192, 16);
      }
      const unsigned x0 = g0[0], x1 = g0[1], y0 = g1[0], y1 = g1[1], e0 = g0[2], e1 = g1[2];
      float mx = fmaxf(__uint_as_float(e0), __uint_as_float(e1));
      mx = fmaxf(mx, dpp<ROR4>(mx)); mx = fmaxf(mx, dpp<ROR8>(mx)); mx = xg_max(mx);
      const float w0 = __builtin_amdgcn_exp2f(__uint_as_float(e0) - mx), w1 = __builtin_amdgcn_exp2f(__uint_as_float(e1) - mx);
      float a[5] = {fmaf(w0, bflo(x0), w1 * bflo(y0)), fmaf(w0, bfhi(x0), w1 * bfhi(y0)), fmaf(w0, bflo(x1), w1 * bflo(y1)), fmaf(w0, bfhi(x1), w1 * bfhi(y1)), w0 + w1};
#pragma unroll
      for (int c = 0; c < 5; ++c) { a[c] += dpp<ROR4>(a[c]); a[c] += dpp<ROR8>(a[c]); a[c] = xg_sum(a[c]); }
      if (lane < 4) {
        *(LAS f32x4*)(lds + L_WA + 32 * (wave * 4 + lane)) = f32x4{a[0], a[1], a[2], a[3]};
        *(LAS f32x2*)(lds + L_WA + 32 * (wave * 4 + lane) + 16) = f32x2{a[4], mx};
      }
    }
    BARRIER();
    if (wave == 2) {
      const f32x4 v = ldsf4(lds + L_WA + 32 * (lane & 31));
      const f32x2 wm = *(const LAS f32x2*)(lds + L_WA + 32 * (lane & 31) + 16);
      float mx = wm[1];
      mx = fmaxf(mx, dpp<ROR4>(mx)); mx = fmaxf(mx, dpp<ROR8>(mx)); mx = xg_max(mx);
      const float w = __builtin_amdgcn_exp2f(wm[1] - mx);
      float a[5] = {w * v[0], w * v[1], w * v[2], w * v[3], w * wm[0]};
#pragma unroll
      for (int c = 0; c < 5; ++c) { a[c] += dpp<ROR4>(a[c]); a[c] += dpp<ROR8>(a[c]); a[c] = xg_sum(a[c]); }
      const float inv = __builtin_amdgcn_rcpf(a[4]);
      const unsigned q0 = qres[0], q1 = qres[1];
      if (lane < 4) *(GAS u32x2*)(out + (4 * (b / 24) + lane) * 96 + 4 * (b % 24)) =
          u32x2{pk(fmaf(a[0], inv, bflo(q0)), fmaf(a[1], inv, bfhi(q0))), pk(fmaf(a[2], inv, bflo(q1)), fmaf(a[3], inv, bfhi(q1)))};
      if (b == 0 && lane == 0) __builtin_amdgcn_raw_buffer_store_b32(tag, rws, 0, WS_EPOCH, 16);
    }
  }
  STAMP(9);
  STAMP_OUT();
}

extern "C" void kernel_launch(void* const* d_in, const int* in_sizes, int n_in, void* d_out, int out_size, void* d_ws, size_t ws_size,
                              hipStream_t stream) {
  k_main<<<NWG, 768, 0, stream>>>((const u16*)d_in[0], (const u16*)d_in[1], (const u16*)d_in[2], (const u16*)d_in[3], (const u16*)d_in[4],
                                  (const u16*)d_in[5], (const u16*)d_in[6], (u16*)d_out, (char*)d_ws);
}
```

```cpp
#define STAMPS 1
#define SW 0
#define SK 0
#define ST 2
#include <hip/hip_runtime.h>
#include <hip/hip_bf16.h>
#pragma clang diagnostic ignored "-Wint-to-pointer-cast"

typedef unsigned short u16;
typedef __attribute__((ext_vector_type(4))) short s16x4;
typedef __attribute__((ext_vector_type(8))) __bf16 bf16x8;
typedef __attribute__((ext_vector_type(2))) __bf16 bf16x2;
typedef __attribute__((ext_vector_type(2))) float f32x2;
typedef __attribute__((ext_vector_type(4))) float f32x4;
typedef __attribute__((ext_vector_type(4))) unsigned u32x4;
typedef __attribute__((ext_vector_type(2))) unsigned u32x2;
#define LAS __attribute__((address_space(3)))
#define GAS __attribute__((address_space(1)))
#define DEVI __device__ __forceinline__

#define PREWRITE 1
#define DRAIN 1
#define WPOL ""
#define WAUX 0
#define VSTART 4
#define CODEPF 8192
constexpr int NWG = 256;
constexpr int NH = 3;
constexpr int NSW = 9;
constexpr int NCONS = 96;
constexpr int REG = 15360;
constexpr int NLATE = 3;
constexpr int L_WQ = (NSW - NLATE) * REG;
constexpr int L_WK = L_WQ + 18432;
constexpr int L_Q = L_WK + 18432;
constexpr int L_WA = NSW * REG;
constexpr int L_QK = L_WA + 18432;
constexpr int L_M = L_QK + 3072;
constexpr int L_L = L_M + 768;
constexpr int L_FL = L_L + 768;
constexpr int L_END = L_FL + 32;
constexpr unsigned INBOX = 16384 + 256;
constexpr unsigned WS_EPOCH = NCONS * INBOX;
constexpr float EPS = 1e-5f;
constexpr float LOG2E = 1.4426950408889634f;
constexpr float THR = 40.f;

DEVI float bflo(unsigned x) { return __uint_as_float(x << 16); }
DEVI float bfhi(unsigned x) { return __uint_as_float(x & 0xffff0000u); }
DEVI unsigned pk(float a, float b) { f32x2 p = {a, b}; bf16x2 h = __builtin_convertvector(p, bf16x2); return __builtin_bit_cast(unsigned, h); }
DEVI f32x4 mma32(u32x4 a, u32x4 b, f32x4 c) { return __builtin_amdgcn_mfma_f32_16x16x32_bf16(__builtin_bit_cast(bf16x8, a), __builtin_bit_cast(bf16x8, b), c, 0, 0, 0); }
DEVI f32x4 mma4(float a, float b, f32x4 c) { return __builtin_amdgcn_mfma_f32_16x16x4f32(a, b, c, 0, 0, 0); }
DEVI float xg_sum(float x) {
  auto s = __builtin_amdgcn_permlane16_swap(__float_as_uint(x), __float_as_uint(x), false, false);
  x = __uint_as_float(s[0]) + __uint_as_float(s[1]);
  auto t = __builtin_amdgcn_permlane32_swap(__float_as_uint(x), __float_as_uint(x), false, false);
  return __uint_as_float(t[0]) + __uint_as_float(t[1]);
}
DEVI float xg_max(float x) {
  auto s = __builtin_amdgcn_permlane16_swap(__float_as_uint(x), __float_as_uint(x), false, false);
  x = fmaxf(__uint_as_float(s[0]), __uint_as_float(s[1]));
  auto t = __builtin_amdgcn_permlane32_swap(__float_as_uint(x), __float_as_uint(x), false, false);
  return fmaxf(__uint_as_float(t[0]), __uint_as_float(t[1]));
}
template <int CTRL> DEVI float dpp(float x) { return __builtin_bit_cast(float, __builtin_amdgcn_mov_dpp(__builtin_bit_cast(int, x), CTRL, 0xf, 0xf, true)); }
constexpr int ROR4 = 0x124, ROR8 = 0x128;
DEVI u32x4 lds128(unsigned a) { return *(const LAS u32x4*)a; }
DEVI f32x4 ldsf4(unsigned a) { return *(const LAS f32x4*)a; }
DEVI u32x2 ldstr(unsigned a) { s16x4 t = __builtin_amdgcn_ds_read_tr16_b64_v4i16((LAS s16x4*)a); return __builtin_bit_cast(u32x2, t); }
DEVI u32x4 ldstr2(unsigned a, unsigned c) { const u32x2 x = ldstr(a), y = ldstr(c); return u32x4{x[0], x[1], y[0], y[1]}; }
DEVI __amdgpu_buffer_rsrc_t rsrc(const void* p, unsigned bytes) { return __builtin_amdgcn_make_buffer_rsrc((void*)p, 0, bytes, 0x00020000); }
#define BARRIER() do { asm volatile("s_waitcnt lgkmcnt(0)" ::: "memory"); __builtin_amdgcn_s_barrier(); asm volatile("" ::: "memory"); } while (0)
#define WAITVM(n) asm volatile("s_waitcnt vmcnt(" #n ")" ::: "memory")
#define PIN(x) asm volatile("" : "+v"(x))
DEVI void post(unsigned a, int lane) {
  asm volatile("s_waitcnt lgkmcnt(0)" ::: "memory");
  if (lane == 0) __hip_atomic_fetch_add((LAS unsigned*)a, 1u, __ATOMIC_RELAXED, __HIP_MEMORY_SCOPE_WORKGROUP);
}
DEVI void await(unsigned a, unsigned n) {
  while (__builtin_amdgcn_readfirstlane(__hip_atomic_load((LAS unsigned*)a, __ATOMIC_RELAXED, __HIP_MEMORY_SCOPE_WORKGROUP)) < n) __builtin_amdgcn_s_sleep(1);
  asm volatile("" ::: "memory");
}
#define DMA3(POL, r, so, la, va, vb, vc) do { unsigned keep_; \
  asm volatile("s_mov_b32 %0, m0\n\ts_mov_b32 m0, %6\n\ts_nop 0\n\t" \
               "buffer_load_dwordx4 %1, %4, %5 offen " POL "lds\n\tbuffer_load_dwordx4 %2, %4, %5 offen offset:1024 " POL "lds\n\t" \
               "buffer_load_dwordx4 %3, %4, %5 offen offset:2048 " POL "lds\n\ts_mov_b32 m0, %0" \
               : "=&s"(keep_) : "v"(va), "v"(vb), "v"(vc), "s"(r), "s"(so), "s"(la) : "memory"); } while (0)

#define STAMP_(k) do { unsigned long long t_, c_; asm volatile("s_memrealtime %0\n\ts_memtime %1\n\ts_waitcnt lgkmcnt(0)" : "=s"(t_), "=s"(c_) :: "memory"); st_[k] = (t_ & 0xffffffffull) | (c_ << 32); } while (0)
#define STAMP(k) do { if (STAMPS == 1 || k == 0 || k == 9) STAMP_(k); } while (0)
#define STAMP_DECL unsigned long long st_[10] = {0,0,0,0,0,0,0,0,0,0}
#define STAMP_OUT() do { if (lane == 0) { unsigned long long* sp_ = (unsigned long long*)(ws + (64u << 20)) + (blockIdx.x * 16 + wave) * 10; for (int k_ = 0; k_ < 10; ++k_) sp_[k_] = st_[k_]; } } while (0)

__global__ void __launch_bounds__(768) k_main(const u16* __restrict__ q, const u16* __restrict__ f0, const u16* __restrict__ f1,
                                              const u16* __restrict__ wq, const u16* __restrict__ wk, const u16* __restrict__ wv,
                                              const u16* __restrict__ wo, u16* out, char* ws) {
  __shared__ __attribute__((aligned(16))) char lds_raw[L_END];
  const unsigned lds = (unsigned)(size_t)(LAS char*)lds_raw;
  const int lane = threadIdx.x & 63, i = lane & 15, g = lane >> 4;
  const int tq = (lane >> 2) & 3, tp = lane & 3;
  const int wave = __builtin_amdgcn_readfirstlane(threadIdx.x >> 6);
  const int b = blockIdx.x;
  const f32x4 z4 = {0.f, 0.f, 0.f, 0.f};
  STAMP_DECL; STAMP(0);
  if (threadIdx.x < 4) *(LAS unsigned*)(lds + L_FL + 4 * threadIdx.x) = 0u;
  BARRIER();
  const __amdgpu_buffer_rsrc_t rws = rsrc(ws, WS_EPOCH + 64);
  u32x2 qres;

  if (wave >= NH) {
    const int s = wave - NH;
    const unsigned base = lds + s * REG;
    const unsigned u = NSW * b + s, yp = (u * 43691u) >> 19;
    const unsigned o0 = u * 6144u + yp * 73728u;
    const unsigned v0 = (lane * 16) ^ (lane >= 48 ? 32 : 0), v1 = (lane * 16) ^ (lane < 32 ? 32 : 0), v2 = (lane * 16) ^ (lane >= 16 ? 32 : 0);
    const __amdgpu_buffer_rsrc_t r0 = rsrc(f0, 384u * 384u * 192u), r1 = rsrc(f1, 192u * 192u * 192u);
    if (s >= NSW - NLATE) await(lds + L_FL + 8, 1);
    DMA3("nt ", r1, u * 3072u, base, v0, v1, v2);
    DMA3("nt ", r0, o0, base + 3072, v0, v1, v2);
    DMA3("nt ", r0, o0 + 73728u, base + 6144, v0, v1, v2);
    DMA3("nt ", r0, o0 + 3072u, base + 9216, v0, v1, v2);
    DMA3("nt ", r0, o0 + 76800u, base + 12288, v0, v1, v2);
    const unsigned aoff = base + 192 * i + 16 * (g ^ (((i >> 2) & 1) << 1));
    const unsigned foff = base + 192 * (i >> 1) + 16 * (g ^ ((i >> 3) << 1));
    const unsigned tb = base + 192 * (4 * g + tq) + 8 * tp;
    const unsigned tE = tb + 32 * (g & 1), tO = tb - 32 * (g & 1);
    const unsigned fb = base + 192 * (2 * g + (tq >> 1)) + 8 * tp;
    const unsigned fE = fb + 32 * (g >> 1), fO = fb - 32 * (g >> 1);
    u32x4 ones = {0x3f803f80u, 0x3f803f80u, 0x3f803f80u, 0x3f803f80u};
    PIN(ones);
    const bool dg = g == (i >> 2);
    const float dmask = dg ? 1.f : 0.f, kneg = dg ? -1.f / 96.f : 0.f;
    const bool b0 = i & 1, b1 = i & 2;
    const float c0 = 96.f * EPS;
    STAMP(1);
    await(lds + L_FL + 8, 1);
    STAMP(2);
    u32x4 qk[3];
#pragma unroll
    for (int k = 0; k < 3; ++k) qk[k] = lds128(lds + L_QK + 1024 * k + 16 * lane);
    f32x4 acc[6];
#pragma unroll
    for (int t = 0; t < 6; ++t) acc[t] = z4;
    float m = 0.f, l = 0.f;
#pragma unroll
    for (int h = 0; h < 2; ++h) {
      if (h == 0) WAITVM(9); else WAITVM(3);
      if (h == 0) STAMP(3);
      u32x4 F[3], At[6];
#pragma unroll
      for (int k = 0; k < 3; ++k) F[k] = lds128(foff + 1536 * h + 64 * k);
#pragma unroll
      for (int t = 0; t < 6; ++t) { const u32x2 y = ldstr(((t & 1) ? fO : fE) + 1536 * h + 32 * t); At[t][2] = y[0]; At[t][3] = y[1]; }
      f32x4 d1 = z4, s1f = z4, gff = {c0, c0, c0, c0};
#pragma unroll
      for (int dy = 0; dy < 2; ++dy) {
        if (dy == 1) { if (h == 0) WAITVM(6); else WAITVM(0); }
        if (dy == 1 && h == 1) STAMP(4);
        const int slot = 3072 * (1 + dy + 2 * h);
        u32x4 X[3];
#pragma unroll
        for (int k = 0; k < 3; ++k) X[k] = lds128(aoff + slot + 64 * k);
#pragma unroll
        for (int t = 0; t < 6; ++t) { const u32x2 y = ldstr(((t & 1) ? tO : tE) + slot + 32 * t); At[t][0] = y[0]; At[t][1] = y[1]; }
        if (dy == 0) {
#pragma unroll
          for (int k = 0; k < 3; ++k) { gff = mma32(F[k], F[k], gff); s1f = mma32(ones, F[k], s1f); d1 = mma32(F[k], qk[k], d1); }
        }
        f32x4 gxx = gff, gxf = z4, s1 = s1f, dl = d1;
#pragma unroll
        for (int k = 0; k < 3; ++k) { gxx = mma32(X[k], X[k], gxx); gxf = mma32(X[k], F[k], gxf); s1 = mma32(ones, X[k], s1); }
#pragma unroll
        for (int k = 0; k < 3; ++k) dl = mma32(X[k], qk[k], dl);
        const float u0 = fmaf(2.f, gxf[0], gxx[0]), u1 = fmaf(2.f, gxf[1], gxx[1]), u2 = fmaf(2.f, gxf[2], gxx[2]), u3 = fmaf(2.f, gxf[3], gxx[3]);
        const float ua = b0 ? u1 : u0, ub = b0 ? u3 : u2, ss = b1 ? ub : ua;
        const float rsm = __builtin_amdgcn_rsqf(fmaxf(fmaf(s1[0] * kneg, s1[0], ss), c0)) * dmask;
        const f32x4 rs = mma4(rsm, 1.f, z4);
        float a[4];
        if (h == 0 && dy == 0) {
#pragma unroll
          for (int r = 0; r < 4; ++r) a[r] = dl[r] * rs[r];
          m = xg_max(fmaxf(fmaxf(a[0], a[1]), fmaxf(a[2], a[3])));
#pragma unroll
          for (int r = 0; r < 4; ++r) a[r] -= m;
        } else {
#pragma unroll
          for (int r = 0; r < 4; ++r) a[r] = fmaf(dl[r], rs[r], -m);
          const float mx = fmaxf(fmaxf(a[0], a[1]), fmaxf(a[2], a[3]));
          if (__builtin_expect(__any(mx > THR), 0)) {
            const float d = fmaxf(xg_max(mx), 0.f), al = __builtin_amdgcn_exp2f(-d);
            m += d; l *= al;
#pragma unroll
            for (int t = 0; t < 6; ++t) acc[t] *= al;
#pragma unroll
            for (int r = 0; r < 4; ++r) a[r] -= d;
          }
        }
        float pp[4];
#pragma unroll
        for (int r = 0; r < 4; ++r) { const float p = __builtin_amdgcn_exp2f(a[r]); l += p; pp[r] = p * rs[r]; }
        const unsigned pa = pk(pp[0], pp[1]), pb = pk(pp[2], pp[3]);
        const u32x4 P = {pa, pb, pa, pb};
#pragma unroll
        for (int t = 0; t < 6; ++t) acc[t] = mma32(At[t], P, acc[t]);
        if (!(h == 1 && dy == 1)) {
#pragma unroll
          for (int t = 0; t < 6; ++t) PIN(acc[t]);
          PIN(l);
        }
      }
    }
    l = xg_sum(l);
#pragma unroll
    for (int t = 0; t < 6; ++t) *(LAS f32x4*)(base + 1024 * t + 16 * lane) = acc[t];
    if (g == 0) { *(LAS float*)(lds + L_M + 48 * i + 4 * s) = m; *(LAS float*)(lds + L_L + 48 * i + 4 * s) = l; }
    STAMP(5);
    BARRIER();
    STAMP(6);
    if (s < 6) {
      const f32x4 m0 = ldsf4(lds + L_M + 48 * i), m1 = ldsf4(lds + L_M + 48 * i + 16);
      const float m8 = *(const LAS float*)(lds + L_M + 48 * i + 32);
      f32x4 pw[NSW];
#pragma unroll
      for (int w = 0; w < NSW; ++w) pw[w] = ldsf4(lds + w * REG + 1024 * s + 16 * lane);
      const float M = fmaxf(fmaxf(fmaxf(fmaxf(m0[0], m0[1]), m0[2]), fmaxf(fmaxf(m0[3], m1[0]), m1[1])), fmaxf(fmaxf(m1[2], m1[3]), m8));
      f32x4 S = __builtin_amdgcn_exp2f(m8 - M) * pw[8];
#pragma unroll
      for (int w = 0; w < 4; ++w) { S += __builtin_amdgcn_exp2f(m0[w] - M) * pw[w]; S += __builtin_amdgcn_exp2f(m1[w] - M) * pw[4 + w]; }
      *(LAS u32x2*)(lds + L_QK + 1024 * (s >> 1) + 16 * lane + 8 * (s & 1)) = u32x2{pk(S[0], S[1]), pk(S[2], S[3])};
    }
    BARRIER();
    STAMP(7);
  } else {
    const int k = wave;
    const unsigned vl = lane * 16;
    if (k < 2) {
      const __amdgpu_buffer_rsrc_t rq = rsrc(q, 3072), rwq = rsrc(wq, 18432), rwk = rsrc(wk, 18432);
      const unsigned so = 9216 * k;
      if (k == 0) DMA3(WPOL, rq, 0, lds + L_Q, vl, vl, vl);
#pragma unroll
      for (int j = 0; j < 3; ++j) DMA3(WPOL, rwq, so + 3072 * j, lds + L_WQ + so + 3072 * j, vl, vl, vl);
#pragma unroll
      for (int j = 0; j < 3; ++j) DMA3(WPOL, rwk, so + 3072 * j, lds + L_WK + so + 3072 * j, vl, vl, vl);
      STAMP(1);
      if (k == 1) {
        WAITVM(9); post(lds + L_FL, lane);
        WAITVM(0); post(lds + L_FL + 4, lane);
      } else {
        WAITVM(18);
        u32x4 qn[3];
        {
          float x[24], sm = 0.f;
#pragma unroll
          for (int s = 0; s < 3; ++s) {
            const u32x4 w4 = lds128(lds + L_Q + 192 * i + 64 * s + 16 * g);
#pragma unroll
            for (int j = 0; j < 4; ++j) { const unsigned w = w4[j]; x[8 * s + 2 * j] = bflo(w); x[8 * s + 2 * j + 1] = bfhi(w); sm += bflo(w) + bfhi(w); }
          }
          const float mean = xg_sum(sm) * (1.f / 96.f);
          float vs = 0.f;
#pragma unroll
          for (int j = 0; j < 24; ++j) { x[j] -= mean; vs = fmaf(x[j], x[j], vs); }
          const float rstd = __builtin_amdgcn_rsqf(xg_sum(vs) * (1.f / 96.f) + EPS);
#pragma unroll
          for (int j = 0; j < 12; ++j) qn[j >> 2][j & 3] = pk(x[2 * j] * rstd, x[2 * j + 1] * rstd);
#pragma unroll
          for (int s = 0; s < 3; ++s) PIN(qn[s]);
        }
        WAITVM(9);
        await(lds + L_FL, 1);
        STAMP(2);
        u32x4 Qb[3];
        const unsigned ta = lds + L_WQ + 192 * (8 * g + tq) + 16 * tp;
#pragma unroll
        for (int kk = 0; kk < 3; ++kk) {
          f32x4 T[2] = {z4, z4};
#pragma unroll
          for (int s = 0; s < 3; ++s)
#pragma unroll
            for (int h = 0; h < 2; ++h) T[h] = mma32(ldstr2(ta + 6144 * s + 64 * kk + 8 * h, ta + 6144 * s + 768 + 64 * kk + 8 * h), qn[s], T[h]);
          Qb[kk] = u32x4{pk(T[0][0], T[0][1]), pk(T[0][2], T[0][3]), pk(T[1][0], T[1][1]), pk(T[1][2], T[1][3])};
          PIN(Qb[kk]);
        }
        WAITVM(0);
        await(lds + L_FL + 4, 1);
        STAMP(3);
        const unsigned wa = lds + L_WK + 192 * (8 * (i >> 2) + (i & 3)) + 16 * g;
        f32x4 D[6]; float cs = 0.f;
#pragma unroll
        for (int t = 0; t < 6; ++t) {
          D[t] = z4;
#pragma unroll
          for (int kk = 0; kk < 3; ++kk) D[t] = mma32(lds128(wa + 6144 * (t >> 1) + 768 * (t & 1) + 64 * kk), Qb[kk], D[t]);
          cs += (D[t][0] + D[t][1]) + (D[t][2] + D[t][3]);
        }
        const float mu = xg_sum(cs) * (LOG2E / 96.f);
#pragma unroll
        for (int s = 0; s < 3; ++s)
          *(LAS u32x4*)(lds + L_QK + 1024 * s + 16 * lane) =
              u32x4{pk(fmaf(D[2 * s][0], LOG2E, -mu), fmaf(D[2 * s][1], LOG2E, -mu)), pk(fmaf(D[2 * s][2], LOG2E, -mu), fmaf(D[2 * s][3], LOG2E, -mu)),
                    pk(fmaf(D[2 * s + 1][0], LOG2E, -mu), fmaf(D[2 * s + 1][1], LOG2E, -mu)), pk(fmaf(D[2 * s + 1][2], LOG2E, -mu), fmaf(D[2 * s + 1][3], LOG2E, -mu))};
        post(lds + L_FL + 8, lane);
      }
    } else {
      unsigned pf = 0;
      if (CODEPF && lane < 4) {
        const unsigned off = (b >> 3) * 512 + lane * 128;
        if (off < CODEPF) pf = *(const GAS unsigned*)((__builtin_amdgcn_s_getpc() & ~0xffull) + off);
      }
      unsigned epoch = __builtin_amdgcn_raw_buffer_load_b32(rws, 0, WS_EPOCH, 16);
      const int c = b < NCONS ? b : 0;
      qres = *(const GAS u32x2*)(q + (4 * (c / 24) + tp) * 96 + 4 * (c % 24));
      PIN(epoch); PIN(qres); PIN(pf);
      if (lane == 0) *(LAS unsigned*)(lds + L_FL + 16) = epoch + 1u;
    }
    if (k == 2) await(lds + L_FL + VSTART, 1);
    STAMP(4);
    u32x4 Bt[2][3];
    {
      const __amdgpu_buffer_rsrc_t rwv = rsrc(wv, 18432), rwo = rsrc(wo, 18432);
      const unsigned so = 6144 * k;
#pragma unroll
      for (int j = 0; j < 2; ++j) DMA3(WPOL, rwv, so + 3072 * j, lds + L_WA + so + 3072 * j, vl, vl, vl);
      const unsigned rowo = 192 * (8 * (i >> 2) + (i & 3)) + 16 * g;
      u32x4 w[6];
#pragma unroll
      for (int j = 0; j < 6; ++j) w[j] = __builtin_amdgcn_raw_buffer_load_b128(rwo, rowo + 768 * (j & 1), 6144 * (j >> 1) + 64 * k, WAUX);
      const unsigned one = (i & 1) ? 0x3f800000u : 0x00003f80u;
#pragma unroll
      for (int h = 0; h < 2; ++h) {
        u32x4 id;
#pragma unroll
        for (int e = 0; e < 4; ++e) id[e] = (g == 2 * h + (i >> 3) && ((i & 7) >> 1) == e) ? one : 0u;
#pragma unroll
        for (int s = 0; s < 3; ++s) {
          const f32x4 a = mma32(w[2 * s], id, z4), c = mma32(w[2 * s + 1], id, z4);
          Bt[h][s] = u32x4{pk(a[0], a[1]), pk(a[2], a[3]), pk(c[0], c[1]), pk(c[2], c[3])};
          PIN(Bt[h][s]);
        }
      }
    }
    WAITVM(0);
    post(lds + L_FL + 12, lane);
    await(lds + L_FL + 12, NH);
    const unsigned tag = *(const LAS unsigned*)(lds + L_FL + 16);
    const unsigned oaddr = (((i >> 2) * 3 + k) * 8 + g) * INBOX + b * 64 + (i & 3) * 16;
    if (PREWRITE) {
      __builtin_amdgcn_raw_buffer_store_b128(u32x4{0u, 0u, 0u, tag - 1u}, rws, oaddr, 0, 16);
      __builtin_amdgcn_raw_buffer_store_b128(u32x4{0u, 0u, 0u, tag - 1u}, rws, oaddr, 4 * INBOX, 16);
    }
    u32x4 W[2][3];
    {
      f32x4 D[2][6];
#pragma unroll
      for (int t = 0; t < 6; ++t) {
        D[0][t] = z4; D[1][t] = z4;
#pragma unroll
        for (int s = 0; s < 3; ++s) {
          const u32x4 A = lds128(lds + L_WA + 3072 * t + 192 * i + 64 * s + 16 * g);
          D[0][t] = mma32(A, Bt[0][s], D[0][t]); D[1][t] = mma32(A, Bt[1][s], D[1][t]);
        }
      }
#pragma unroll
      for (int h = 0; h < 2; ++h) {
        float cs = 0.f;
#pragma unroll
        for (int t = 0; t < 6; ++t) cs += (D[h][t][0] + D[h][t][1]) + (D[h][t][2] + D[h][t][3]);
        const float sc = 9.797958971132712f, mu = xg_sum(cs) * (sc / 96.f);
#pragma unroll
        for (int t = 0; t < 6; ++t) {
          W[h][t >> 1][2 * (t & 1)] = pk(fmaf(D[h][t][0], sc, -mu), fmaf(D[h][t][1], sc, -mu));
          W[h][t >> 1][2 * (t & 1) + 1] = pk(fmaf(D[h][t][2], sc, -mu), fmaf(D[h][t][3], sc, -mu));
        }
#pragma unroll
        for (int s = 0; s < 3; ++s) PIN(W[h][s]);
      }
    }
    WAITVM(0);
    STAMP(5);
    BARRIER();
    float lse, inv;
    {
      const f32x4 m0 = ldsf4(lds + L_M + 48 * i), m1 = ldsf4(lds + L_M + 48 * i + 16), l0 = ldsf4(lds + L_L + 48 * i), l1 = ldsf4(lds + L_L + 48 * i + 16);
      const float m8 = *(const LAS float*)(lds + L_M + 48 * i + 32), l8 = *(const LAS float*)(lds + L_L + 48 * i + 32);
      const float M = fmaxf(fmaxf(fmaxf(fmaxf(m0[0], m0[1]), m0[2]), fmaxf(fmaxf(m0[3], m1[0]), m1[1])), fmaxf(fmaxf(m1[2], m1[3]), m8));
      float L = __builtin_amdgcn_exp2f(m8 - M) * l8;
#pragma unroll
      for (int w = 0; w < 4; ++w) { L = fmaf(__builtin_amdgcn_exp2f(m0[w] - M), l0[w], L); L = fmaf(__builtin_amdgcn_exp2f(m1[w] - M), l1[w], L); }
      lse = M + __builtin_amdgcn_logf(L); inv = __builtin_amdgcn_rcpf(L);
      PIN(lse); PIN(inv);
    }
    BARRIER();
    STAMP(6);
    u32x4 S[3];
#pragma unroll
    for (int s = 0; s < 3; ++s) S[s] = lds128(lds + L_QK + 1024 * s + 16 * lane);
#pragma unroll
    for (int h = 0; h < 2; ++h) {
      f32x4 T = z4;
#pragma unroll
      for (int s = 0; s < 3; ++s) T = mma32(W[h][s], S[s], T);
      const u32x4 gr = {pk(T[0] * inv, T[1] * inv), pk(T[2] * inv, T[3] * inv), __float_as_uint(lse), tag};
      __builtin_amdgcn_raw_buffer_store_b128(gr, rws, oaddr, 4 * INBOX * h, 16);
    }
    STAMP(7);
    if (DRAIN && b < NCONS) WAITVM(0);
  }
  if (b < NCONS) {
    const unsigned tag = *(const LAS unsigned*)(lds + L_FL + 16);
    const unsigned ia = b * INBOX + wave * 1024 + lane * 16;
    BARRIER();
    STAMP(8);
    if (wave < 8) {
      u32x4 g0 = __builtin_amdgcn_raw_buffer_load_b128(rws, ia, 0, 16), g1 = __builtin_amdgcn_raw_buffer_load_b128(rws, ia, 8192, 16);
      for (unsigned spin = 0; spin < (1u << 20); ++spin) {
        const bool m0 = g0[3] != tag, m1 = g1[3] != tag;
        if (!__any(m0 | m1)) break;
        asm volatile("" ::: "memory");
        if (m0) g0 = __builtin_amdgcn_raw_buffer_load_b128(rws, ia, 0, 16);
        if (m1) g1 = __builtin_amdgcn_raw_buffer_load_b128(rws, ia, 8192, 16);
      }
      const unsigned x0 = g0[0], x1 = g0[1], y0 = g1[0], y1 = g1[1], e0 = g0[2], e1 = g1[2];
      float mx = fmaxf(__uint_as_float(e0), __uint_as_float(e1));
      mx = fmaxf(mx, dpp<ROR4>(mx)); mx = fmaxf(mx, dpp<ROR8>(mx)); mx = xg_max(mx);
      const float w0 = __builtin_amdgcn_exp2f(__uint_as_float(e0) - mx), w1 = __builtin_amdgcn_exp2f(__uint_as_float(e1) - mx);
      float a[5] = {fmaf(w0, bflo(x0), w1 * bflo(y0)), fmaf(w0, bfhi(x0), w1 * bfhi(y0)), fmaf(w0, bflo(x1), w1 * bflo(y1)), fmaf(w0, bfhi(x1), w1 * bfhi(y1)), w0 + w1};
#pragma unroll
      for (int c = 0; c < 5; ++c) { a[c] += dpp<ROR4>(a[c]); a[c] += dpp<ROR8>(a[c]); a[c] = xg_sum(a[c]); }
      if (lane < 4) {
        *(LAS f32x4*)(lds + L_WA + 32 * (wave * 4 + lane)) = f32x4{a[0], a[1], a[2], a[3]};
        *(LAS f32x2*)(lds + L_WA + 32 * (wave * 4 + lane) + 16) = f32x2{a[4], mx};
      }
    }
    BARRIER();
    if (wave == 2) {
      const f32x4 v = ldsf4(lds + L_WA + 32 * (lane & 31));
      const f32x2 wm = *(const LAS f32x2*)(lds + L_WA + 32 * (lane & 31) + 16);
      float mx = wm[1];
      mx = fmaxf(mx, dpp<ROR4>(mx)); mx = fmaxf(mx, dpp<ROR8>(mx)); mx = xg_max(mx);
      const float w = __builtin_amdgcn_exp2f(wm[1] - mx);
      float a[5] = {w * v[0], w * v[1], w * v[2], w * v[3], w * wm[0]};
#pragma unroll
      for (int c = 0; c < 5; ++c) { a[c] += dpp<ROR4>(a[c]); a[c] += dpp<ROR8>(a[c]); a[c] = xg_sum(a[c]); }
      const float inv = __builtin_amdgcn_rcpf(a[4]);
      const unsigned q0 = qres[0], q1 = qres[1];
      if (lane < 4) *(GAS u32x2*)(out + (4 * (b / 24) + lane) * 96 + 4 * (b % 24)) =
          u32x2{pk(fmaf(a[0], inv, bflo(q0)), fmaf(a[1], inv, bfhi(q0))), pk(fmaf(a[2], inv, bflo(q1)), fmaf(a[3], inv, bfhi(q1)))};
      if (b == 0 && lane == 0) __builtin_amdgcn_raw_buffer_store_b32(tag, rws, 0, WS_EPOCH, 16);
    }
  }
  STAMP(9);
  STAMP_OUT();
}

__global__ void __launch_bounds__(256) k_spin(const unsigned long long* ts) {
  __shared__ unsigned r[4][256];
  const int b = threadIdx.x;
  unsigned mn0 = ~0u;
  for (int w = 0; w < 12; ++w) mn0 = min(mn0, (unsigned)ts[(b * 16 + w) * 10]);
  const unsigned x = (unsigned)ts[(b * 16 + SW) * 10 + SK];
  unsigned xm = x;
  if (SK == 9) for (int w = 0; w < 12; ++w) xm = max(xm, (unsigned)ts[(b * 16 + w) * 10 + 9]);
  r[0][b] = mn0; __syncthreads();
  for (int o = 128; o > 0; o >>= 1) { if (b < o) r[0][b] = min(r[0][b], r[0][b + o]); __syncthreads(); }
  const unsigned t0 = r[0][0];
  const bool v = x != 0;
  r[1][b] = v ? xm - t0 : 0; r[2][b] = v ? xm - t0 : 0; r[3][b] = v;
  __syncthreads();
  for (int o = 128; o > 0; o >>= 1) { if (b < o) { r[1][b] = max(r[1][b], r[1][b + o]); r[2][b] += r[2][b + o]; r[3][b] += r[3][b + o]; } __syncthreads(); }
  unsigned v0 = r[1][0] * 20, v1 = r[2][0] * 20 / r[3][0], v2 = 0;
  asm volatile("" : "+v"(v0), "+v"(v1), "+v"(v2));
  const unsigned long long until = __builtin_amdgcn_s_memrealtime() + (ST == 0 ? v0 : ST == 1 ? v1 : v2);
  while (__builtin_amdgcn_s_memrealtime() < until) ;
}

extern "C" void kernel_launch(void* const* d_in, const int* in_sizes, int n_in, void* d_out, int out_size, void* d_ws, size_t ws_size,
                              hipStream_t stream) {
  k_main<<<NWG, 768, 0, stream>>>((const u16*)d_in[0], (const u16*)d_in[1], (const u16*)d_in[2], (const u16*)d_in[3], (const u16*)d_in[4],
                                  (const u16*)d_in[5], (const u16*)d_in[6], (u16*)d_out, (char*)d_ws);
  k_spin<<<1, 256, 0, stream>>>((const unsigned long long*)((char*)d_ws + (64u << 20)));
}
```

```cpp
#define STAMPS 2
#define ST 0
#include <hip/hip_runtime.h>
#include <hip/hip_bf16.h>
#pragma clang diagnostic ignored "-Wint-to-pointer-cast"

typedef unsigned short u16;
typedef __attribute__((ext_vector_type(4))) short s16x4;
typedef __attribute__((ext_vector_type(8))) __bf16 bf16x8;
typedef __attribute__((ext_vector_type(2))) __bf16 bf16x2;
typedef __attribute__((ext_vector_type(2))) float f32x2;
typedef __attribute__((ext_vector_type(4))) float f32x4;
typedef __attribute__((ext_vector_type(4))) unsigned u32x4;
typedef __attribute__((ext_vector_type(2))) unsigned u32x2;
#define LAS __attribute__((address_space(3)))
#define GAS __attribute__((address_space(1)))
#define DEVI __device__ __forceinline__

#define PREWRITE 1
#define DRAIN 1
#define WPOL ""
#define WAUX 0
#define VSTART 4
#define CODEPF 8192
constexpr int NWG = 256;
constexpr int NH = 3;
constexpr int NSW = 9;
constexpr int NCONS = 96;
constexpr int REG = 15360;
constexpr int NLATE = 3;
constexpr int L_WQ = (NSW - NLATE) * REG;
constexpr int L_WK = L_WQ + 18432;
constexpr int L_Q = L_WK + 18432;
constexpr int L_WA = NSW * REG;
constexpr int L_QK = L_WA + 18432;
constexpr int L_M = L_QK + 3072;
constexpr int L_L = L_M + 768;
constexpr int L_FL = L_L + 768;
constexpr int L_END = L_FL + 32;
constexpr unsigned INBOX = 16384 + 256;
constexpr unsigned WS_EPOCH = NCONS * INBOX;
constexpr float EPS = 1e-5f;
constexpr float LOG2E = 1.4426950408889634f;
constexpr float THR = 40.f;

DEVI float bflo(unsigned x) { return __uint_as_float(x << 16); }
DEVI float bfhi(unsigned x) { return __uint_as_float(x & 0xffff0000u); }
DEVI unsigned pk(float a, float b) { f32x2 p = {a, b}; bf16x2 h = __builtin_convertvector(p, bf16x2); return __builtin_bit_cast(unsigned, h); }
DEVI f32x4 mma32(u32x4 a, u32x4 b, f32x4 c) { return __builtin_amdgcn_mfma_f32_16x16x32_bf16(__builtin_bit_cast(bf16x8, a), __builtin_bit_cast(bf16x8, b), c, 0, 0, 0); }
DEVI f32x4 mma4(float a, float b, f32x4 c) { return __builtin_amdgcn_mfma_f32_16x16x4f32(a, b, c, 0, 0, 0); }
DEVI float xg_sum(float x) {
  auto s = __builtin_amdgcn_permlane16_swap(__float_as_uint(x), __float_as_uint(x), false, false);
  x = __uint_as_float(s[0]) + __uint_as_float(s[1]);
  auto t = __builtin_amdgcn_permlane32_swap(__float_as_uint(x), __float_as_uint(x), false, false);
  return __uint_as_float(t[0]) + __uint_as_float(t[1]);
}
DEVI float xg_max(float x) {
  auto s = __builtin_amdgcn_permlane16_swap(__float_as_uint(x), __float_as_uint(x), false, false);
  x = fmaxf(__uint_as_float(s[0]), __uint_as_float(s[1]));
  auto t = __builtin_amdgcn_permlane32_swap(__float_as_uint(x), __float_as_uint(x), false, false);
  return fmaxf(__uint_as_float(t[0]), __uint_as_float(t[1]));
}
template <int CTRL> DEVI float dpp(float x) { return __builtin_bit_cast(float, __builtin_amdgcn_mov_dpp(__builtin_bit_cast(int, x), CTRL, 0xf, 0xf, true)); }
constexpr int ROR4 = 0x124, ROR8 = 0x128;
DEVI u32x4 lds128(unsigned a) { return *(const LAS u32x4*)a; }
DEVI f32x4 ldsf4(unsigned a) { return *(const LAS f32x4*)a; }
DEVI u32x2 ldstr(unsigned a) { s16x4 t = __builtin_amdgcn_ds_read_tr16_b64_v4i16((LAS s16x4*)a); return __builtin_bit_cast(u32x2, t); }
DEVI u32x4 ldstr2(unsigned a, unsigned c) { const u32x2 x = ldstr(a), y = ldstr(c); return u32x4{x[0], x[1], y[0], y[1]}; }
DEVI __amdgpu_buffer_rsrc_t rsrc(const void* p, unsigned bytes) { return __builtin_amdgcn_make_buffer_rsrc((void*)p, 0, bytes, 0x00020000); }
#define BARRIER() do { asm volatile("s_waitcnt lgkmcnt(0)" ::: "memory"); __builtin_amdgcn_s_barrier(); asm volatile("" ::: "memory"); } while (0)
#define WAITVM(n) asm volatile("s_waitcnt vmcnt(" #n ")" ::: "memory")
#define PIN(x) asm volatile("" : "+v"(x))
DEVI void post(unsigned a, int lane) {
  asm volatile("s_waitcnt lgkmcnt(0)" ::: "memory");
  if (lane == 0) __hip_atomic_fetch_add((LAS unsigned*)a, 1u, __ATOMIC_RELAXED, __HIP_MEMORY_SCOPE_WORKGROUP);
}
DEVI void await(unsigned a, unsigned n) {
  while (__builtin_amdgcn_readfirstlane(__hip_atomic_load((LAS unsigned*)a, __ATOMIC_RELAXED, __HIP_MEMORY_SCOPE_WORKGROUP)) < n) __builtin_amdgcn_s_sleep(1);
  asm volatile("" ::: "memory");
}
#define DMA3(POL, r, so, la, va, vb, vc) do { unsigned keep_; \
  asm volatile("s_mov_b32 %0, m0\n\ts_mov_b32 m0, %6\n\ts_nop 0\n\t" \
               "buffer_load_dwordx4 %1, %4, %5 offen " POL "lds\n\tbuffer_load_dwordx4 %2, %4, %5 offen offset:1024 " POL "lds\n\t" \
               "buffer_load_dwordx4 %3, %4, %5 offen offset:2048 " POL "lds\n\ts_mov_b32 m0, %0" \
               : "=&s"(keep_) : "v"(va), "v"(vb), "v"(vc), "s"(r), "s"(so), "s"(la) : "memory"); } while (0)

#define STAMP_(k) do { unsigned long long t_, c_; asm volatile("s_memrealtime %0\n\ts_memtime %1\n\ts_waitcnt lgkmcnt(0)" : "=s"(t_), "=s"(c_) :: "memory"); st_[k] = (t_ & 0xffffffffull) | (c_ << 32); } while (0)
#define STAMP(k) do { if (STAMPS == 1 || k == 0 || k == 9) STAMP_(k); } while (0)
#define STAMP_DECL unsigned long long st_[10] = {0,0,0,0,0,0,0,0,0,0}
#define STAMP_OUT() do { if (lane == 0) { unsigned long long* sp_ = (unsigned long long*)(ws + (64u << 20)) + (blockIdx.x * 16 + wave) * 10; for (int k_ = 0; k_ < 10; ++k_) sp_[k_] = st_[k_]; } } while (0)

__global__ void __launch_bounds__(768) k_main(const u16* __restrict__ q, const u16* __restrict__ f0, const u16* __restrict__ f1,
                                              const u16* __restrict__ wq, const u16* __restrict__ wk, const u16* __restrict__ wv,
                                              const u16* __restrict__ wo, u16* out, char* ws) {
  __shared__ __attribute__((aligned(16))) char lds_raw[L_END];
  const unsigned lds = (unsigned)(size_t)(LAS char*)lds_raw;
  const int lane = threadIdx.x & 63, i = lane & 15, g = lane >> 4;
  const int tq = (lane >> 2) & 3, tp = lane & 3;
  const int wave = __builtin_amdgcn_readfirstlane(threadIdx.x >> 6);
  const int b = blockIdx.x;
  const f32x4 z4 = {0.f, 0.f, 0.f, 0.f};
  STAMP_DECL; STAMP(0);
  if (threadIdx.x < 4) *(LAS unsigned*)(lds + L_FL + 4 * threadIdx.x) = 0u;
  BARRIER();
  const __amdgpu_buffer_rsrc_t rws = rsrc(ws, WS_EPOCH + 64);
  u32x2 qres;

  if (wave >= NH) {
    const int s = wave - NH;
    const unsigned base = lds + s * REG;
    const unsigned u = NSW * b + s, yp = (u * 43691u) >> 19;
    const unsigned o0 = u * 6144u + yp * 73728u;
    const unsigned v0 = (lane * 16) ^ (lane >= 48 ? 32 : 0), v1 = (lane * 16) ^ (lane < 32 ? 32 : 0), v2 = (lane * 16) ^ (lane >= 16 ? 32 : 0);
    const __amdgpu_buffer_rsrc_t r0 = rsrc(f0, 384u * 384u * 192u), r1 = rsrc(f1, 192u * 192u * 192u);
    if (s >= NSW - NLATE) await(lds + L_FL + 8, 1);
    DMA3("nt ", r1, u * 3072u, base, v0, v1, v2);
    DMA3("nt ", r0, o0, base + 3072, v0, v1, v2);
    DMA3("nt ", r0, o0 + 73728u, base + 6144, v0, v1, v2);
    DMA3("nt ", r0, o0 + 3072u, base + 9216, v0, v1, v2);
    DMA3("nt ", r0, o0 + 76800u, base + 12288, v0, v1, v2);
    const unsigned aoff = base + 192 * i + 16 * (g ^ (((i >> 2) & 1) << 1));
    const unsigned foff = base + 192 * (i >> 1) + 16 * (g ^ ((i >> 3) << 1));
    const unsigned tb = base + 192 * (4 * g + tq) + 8 * tp;
    const unsigned tE = tb + 32 * (g & 1), tO = tb - 32 * (g & 1);
    const unsigned fb = base + 192 * (2 * g + (tq >> 1)) + 8 * tp;
    const unsigned fE = fb + 32 * (g >> 1), fO = fb - 32 * (g >> 1);
    u32x4 ones = {0x3f803f80u, 0x3f803f80u, 0x3f803f80u, 0x3f803f80u};
    PIN(ones);
    const bool dg = g == (i >> 2);
    const float dmask = dg ? 1.f : 0.f, kneg = dg ? -1.f / 96.f : 0.f;
    const bool b0 = i & 1, b1 = i & 2;
    const float c0 = 96.f * EPS;
    STAMP(1);
    await(lds + L_FL + 8, 1);
    STAMP(2);
    u32x4 qk[3];
#pragma unroll
    for (int k = 0; k < 3; ++k) qk[k] = lds128(lds + L_QK + 1024 * k + 16 * lane);
    f32x4 acc[6];
#pragma unroll
    for (int t = 0; t < 6; ++t) acc[t] = z4;
    float m = 0.f, l = 0.f;
#pragma unroll
    for (int h = 0; h < 2; ++h) {
      if (h == 0) WAITVM(9); else WAITVM(3);
      if (h == 0) STAMP(3);
      u32x4 F[3], At[6];
#pragma unroll
      for (int k = 0; k < 3; ++k) F[k] = lds128(foff + 1536 * h + 64 * k);
#pragma unroll
      for (int t = 0; t < 6; ++t) { const u32x2 y = ldstr(((t & 1) ? fO : fE) + 1536 * h + 32 * t); At[t][2] = y[0]; At[t][3] = y[1]; }
      f32x4 d1 = z4, s1f = z4, gff = {c0, c0, c0, c0};
#pragma unroll
      for (int dy = 0; dy < 2; ++dy) {
        if (dy == 1) { if (h == 0) WAITVM(6); else WAITVM(0); }
        if (dy == 1 && h == 1) STAMP(4);
        const int slot = 3072 * (1 + dy + 2 * h);
        u32x4 X[3];
#pragma unroll
        for (int k = 0; k < 3; ++k) X[k] = lds128(aoff + slot + 64 * k);
#pragma unroll
        for (int t = 0; t < 6; ++t) { const u32x2 y = ldstr(((t & 1) ? tO : tE) + slot + 32 * t); At[t][0] = y[0]; At[t][1] = y[1]; }
        if (dy == 0) {
#pragma unroll
          for (int k = 0; k < 3; ++k) { gff = mma32(F[k], F[k], gff); s1f = mma32(ones, F[k], s1f); d1 = mma32(F[k], qk[k], d1); }
        }
        f32x4 gxx = gff, gxf = z4, s1 = s1f, dl = d1;
#pragma unroll
        for (int k = 0; k < 3; ++k) { gxx = mma32(X[k], X[k], gxx); gxf = mma32(X[k], F[k], gxf); s1 = mma32(ones, X[k], s1); }
#pragma unroll
        for (int k = 0; k < 3; ++k) dl = mma32(X[k], qk[k], dl);
        const float u0 = fmaf(2.f, gxf[0], gxx[0]), u1 = fmaf(2.f, gxf[1], gxx[1]), u2 = fmaf(2.f, gxf[2], gxx[2]), u3 = fmaf(2.f, gxf[3], gxx[3]);
        const float ua = b0 ? u1 : u0, ub = b0 ? u3 : u2, ss = b1 ? ub : ua;
        const float rsm = __builtin_amdgcn_rsqf(fmaxf(fmaf(s1[0] * kneg, s1[0], ss), c0)) * dmask;
        const f32x4 rs = mma4(rsm, 1.f, z4);
        float a[4];
        if (h == 0 && dy == 0) {
#pragma unroll
          for (int r = 0; r < 4; ++r) a[r] = dl[r] * rs[r];
          m = xg_max(fmaxf(fmaxf(a[0], a[1]), fmaxf(a[2], a[3])));
#pragma unroll
          for (int r = 0; r < 4; ++r) a[r] -= m;
        } else {
#pragma unroll
          for (int r = 0; r < 4; ++r) a[r] = fmaf(dl[r], rs[r], -m);
          const float mx = fmaxf(fmaxf(a[0], a[1]), fmaxf(a[2], a[3]));
          if (__builtin_expect(__any(mx > THR), 0)) {
            const float d = fmaxf(xg_max(mx), 0.f), al = __builtin_amdgcn_exp2f(-d);
            m += d; l *= al;
#pragma unroll
            for (int t = 0; t < 6; ++t) acc[t] *= al;
#pragma unroll
            for (int r = 0; r < 4; ++r) a[r] -= d;
          }
        }
        float pp[4];
#pragma unroll
        for (int r = 0; r < 4; ++r) { const float p = __builtin_amdgcn_exp2f(a[r]); l += p; pp[r] = p * rs[r]; }
        const unsigned pa = pk(pp[0], pp[1]), pb = pk(pp[2], pp[3]);
        const u32x4 P = {pa, pb, pa, pb};
#pragma unroll
        for (int t = 0; t < 6; ++t) acc[t] = mma32(At[t], P, acc[t]);
        if (!(h == 1 && dy == 1)) {
#pragma unroll
          for (int t = 0; t < 6; ++t) PIN(acc[t]);
          PIN(l);
        }
      }
    }
    l = xg_sum(l);
#pragma unroll
    for (int t = 0; t < 6; ++t) *(LAS f32x4*)(base + 1024 * t + 16 * lane) = acc[t];
    if (g == 0) { *(LAS float*)(lds + L_M + 48 * i + 4 * s) = m; *(LAS float*)(lds + L_L + 48 * i + 4 * s) = l; }
    STAMP(5);
    BARRIER();
    STAMP(6);
    if (s < 6) {
      const f32x4 m0 = ldsf4(lds + L_M + 48 * i), m1 = ldsf4(lds + L_M + 48 * i + 16);
      const float m8 = *(const LAS float*)(lds + L_M + 48 * i + 32);
      f32x4 pw[NSW];
#pragma unroll
      for (int w = 0; w < NSW; ++w) pw[w] = ldsf4(lds + w * REG + 1024 * s + 16 * lane);
      const float M = fmaxf(fmaxf(fmaxf(fmaxf(m0[0], m0[1]), m0[2]), fmaxf(fmaxf(m0[3], m1[0]), m1[1])), fmaxf(fmaxf(m1[2], m1[3]), m8));
      f32x4 S = __builtin_amdgcn_exp2f(m8 - M) * pw[8];
#pragma unroll
      for (int w = 0; w < 4; ++w) { S += __builtin_amdgcn_exp2f(m0[w] - M) * pw[w]; S += __builtin_amdgcn_exp2f(m1[w] - M) * pw[4 + w]; }
      *(LAS u32x2*)(lds + L_QK + 1024 * (s >> 1) + 16 * lane + 8 * (s & 1)) = u32x2{pk(S[0], S[1]), pk(S[2], S[3])};
    }
    BARRIER();
    STAMP(7);
  } else {
    const int k = wave;
    const unsigned vl = lane * 16;
    if (k < 2) {
      const __amdgpu_buffer_rsrc_t rq = rsrc(q, 3072), rwq = rsrc(wq, 18432), rwk = rsrc(wk, 18432);
      const unsigned so = 9216 * k;
      if (k == 0) DMA3(WPOL, rq, 0, lds + L_Q, vl, vl, vl);
#pragma unroll
      for (int j = 0; j < 3; ++j) DMA3(WPOL, rwq, so + 3072 * j, lds + L_WQ + so + 3072 * j, vl, vl, vl);
#pragma unroll
      for (int j = 0; j < 3; ++j) DMA3(WPOL, rwk, so + 3072 * j, lds + L_WK + so + 3072 * j, vl, vl, vl);
      STAMP(1);
      if (k == 1) {
        WAITVM(9); post(lds + L_FL, lane);
        WAITVM(0); post(lds + L_FL + 4, lane);
      } else {
        WAITVM(18);
        u32x4 qn[3];
        {
          float x[24], sm = 0.f;
#pragma unroll
          for (int s = 0; s < 3; ++s) {
            const u32x4 w4 = lds128(lds + L_Q + 192 * i + 64 * s + 16 * g);
#pragma unroll
            for (int j = 0; j < 4; ++j) { const unsigned w = w4[j]; x[8 * s + 2 * j] = bflo(w); x[8 * s + 2 * j + 1] = bfhi(w); sm += bflo(w) + bfhi(w); }
          }
          const float mean = xg_sum(sm) * (1.f / 96.f);
          float vs = 0.f;
#pragma unroll
          for (int j = 0; j < 24; ++j) { x[j] -= mean; vs = fmaf(x[j], x[j], vs); }
          const float rstd = __builtin_amdgcn_rsqf(xg_sum(vs) * (1.f / 96.f) + EPS);
#pragma unroll
          for (int j = 0; j < 12; ++j) qn[j >> 2][j & 3] = pk(x[2 * j] * rstd, x[2 * j + 1] * rstd);
#pragma unroll
          for (int s = 0; s < 3; ++s) PIN(qn[s]);
        }
        WAITVM(9);
        await(lds + L_FL, 1);
        STAMP(2);
        u32x4 Qb[3];
        const unsigned ta = lds + L_WQ + 192 * (8 * g + tq) + 16 * tp;
#pragma unroll
        for (int kk = 0; kk < 3; ++kk) {
          f32x4 T[2] = {z4, z4};
#pragma unroll
          for (int s = 0; s < 3; ++s)
#pragma unroll
            for (int h = 0; h < 2; ++h) T[h] = mma32(ldstr2(ta + 6144 * s + 64 * kk + 8 * h, ta + 6144 * s + 768 + 64 * kk + 8 * h), qn[s], T[h]);
          Qb[kk] = u32x4{pk(T[0][0], T[0][1]), pk(T[0][2], T[0][3]), pk(T[1][0], T[1][1]), pk(T[1][2], T[1][3])};
          PIN(Qb[kk]);
        }
        WAITVM(0);
        await(lds + L_FL + 4, 1);
        STAMP(3);
        const unsigned wa = lds + L_WK + 192 * (8 * (i >> 2) + (i & 3)) + 16 * g;
        f32x4 D[6]; float cs = 0.f;
#pragma unroll
        for (int t = 0; t < 6; ++t) {
          D[t] = z4;
#pragma unroll
          for (int kk = 0; kk < 3; ++kk) D[t] = mma32(lds128(wa + 6144 * (t >> 1) + 768 * (t & 1) + 64 * kk), Qb[kk], D[t]);
          cs += (D[t][0] + D[t][1]) + (D[t][2] + D[t][3]);
        }
        const float mu = xg_sum(cs) * (LOG2E / 96.f);
#pragma unroll
        for (int s = 0; s < 3; ++s)
          *(LAS u32x4*)(lds + L_QK + 1024 * s + 16 * lane) =
              u32x4{pk(fmaf(D[2 * s][0], LOG2E, -mu), fmaf(D[2 * s][1], LOG2E, -mu)), pk(fmaf(D[2 * s][2], LOG2E, -mu), fmaf(D[2 * s][3], LOG2E, -mu)),
                    pk(fmaf(D[2 * s + 1][0], LOG2E, -mu), fmaf(D[2 * s + 1][1], LOG2E, -mu)), pk(fmaf(D[2 * s + 1][2], LOG2E, -mu), fmaf(D[2 * s + 1][3], LOG2E, -mu))};
        post(lds + L_FL + 8, lane);
      }
    } else {
      unsigned pf = 0;
      if (CODEPF && lane < 4) {
        const unsigned off = (b >> 3) * 512 + lane * 128;
        if (off < CODEPF) pf = *(const GAS unsigned*)((__builtin_amdgcn_s_getpc() & ~0xffull) + off);
      }
      unsigned epoch = __builtin_amdgcn_raw_buffer_load_b32(rws, 0, WS_EPOCH, 16);
      const int c = b < NCONS ? b : 0;
      qres = *(const GAS u32x2*)(q + (4 * (c / 24) + tp) * 96 + 4 * (c % 24));
      PIN(epoch); PIN(qres); PIN(pf);
      if (lane == 0) *(LAS unsigned*)(lds + L_FL + 16) = epoch + 1u;
    }
    if (k == 2) await(lds + L_FL + VSTART, 1);
    STAMP(4);
    u32x4 Bt[2][3];
    {
      const __amdgpu_buffer_rsrc_t rwv = rsrc(wv, 18432), rwo = rsrc(wo, 18432);
      const unsigned so = 6144 * k;
#pragma unroll
      for (int j = 0; j < 2; ++j) DMA3(WPOL, rwv, so + 3072 * j, lds + L_WA + so + 3072 * j, vl, vl, vl);
      const unsigned rowo = 192 * (8 * (i >> 2) + (i & 3)) + 16 * g;
      u32x4 w[6];
#pragma unroll
      for (int j = 0; j < 6; ++j) w[j] = __builtin_amdgcn_raw_buffer_load_b128(rwo, rowo + 768 * (j & 1), 6144 * (j >> 1) + 64 * k, WAUX);
      const unsigned one = (i & 1) ? 0x3f800000u : 0x00003f80u;
#pragma unroll
      for (int h = 0; h < 2; ++h) {
        u32x4 id;
#pragma unroll
        for (int e = 0; e < 4; ++e) id[e] = (g == 2 * h + (i >> 3) && ((i & 7) >> 1) == e) ? one : 0u;
#pragma unroll
        for (int s = 0; s < 3; ++s) {
          const f32x4 a = mma32(w[2 * s], id, z4), c = mma32(w[2 * s + 1], id, z4);
          Bt[h][s] = u32x4{pk(a[0], a[1]), pk(a[2], a[3]), pk(c[0], c[1]), pk(c[2], c[3])};
          PIN(Bt[h][s]);
        }
      }
    }
    WAITVM(0);
    post(lds + L_FL + 12, lane);
    await(lds + L_FL + 12, NH);
    const unsigned tag = *(const LAS unsigned*)(lds + L_FL + 16);
    const unsigned oaddr = (((i >> 2) * 3 + k) * 8 + g) * INBOX + b * 64 + (i & 3) * 16;
    if (PREWRITE) {
      __builtin_amdgcn_raw_buffer_store_b128(u32x4{0u, 0u, 0u, tag - 1u}, rws, oaddr, 0, 16);
      __builtin_amdgcn_raw_buffer_store_b128(u32x4{0u, 0u, 0u, tag - 1u}, rws, oaddr, 4 * INBOX, 16);
    }
    u32x4 W[2][3];
    {
      f32x4 D[2][6];
#pragma unroll
      for (int t = 0; t < 6; ++t) {
        D[0][t] = z4; D[1][t] = z4;
#pragma unroll
        for (int s = 0; s < 3; ++s) {
          const u32x4 A = lds128(lds + L_WA + 3072 * t + 192 * i + 64 * s + 16 * g);
          D[0][t] = mma32(A, Bt[0][s], D[0][t]); D[1][t] = mma32(A, Bt[1][s], D[1][t]);
        }
      }
#pragma unroll
      for (int h = 0; h < 2; ++h) {
        float cs = 0.f;
#pragma unroll
        for (int t = 0; t < 6; ++t) cs += (D[h][t][0] + D[h][t][1]) + (D[h][t][2] + D[h][t][3]);
        const float sc = 9.797958971132712f, mu = xg_sum(cs) * (sc / 96.f);
#pragma unroll
        for (int t = 0; t < 6; ++t) {
          W[h][t >> 1][2 * (t & 1)] = pk(fmaf(D[h][t][0], sc, -mu), fmaf(D[h][t][1], sc, -mu));
          W[h][t >> 1][2 * (t & 1) + 1] = pk(fmaf(D[h][t][2], sc, -mu), fmaf(D[h][t][3], sc, -mu));
        }
#pragma unroll
        for (int s = 0; s < 3; ++s) PIN(W[h][s]);
      }
    }
    WAITVM(0);
    STAMP(5);
    BARRIER();
    float lse, inv;
    {
      const f32x4 m0 = ldsf4(lds + L_M + 48 * i), m1 = ldsf4(lds + L_M + 48 * i + 16), l0 = ldsf4(lds + L_L + 48 * i), l1 = ldsf4(lds + L_L + 48 * i + 16);
      const float m8 = *(const LAS float*)(lds + L_M + 48 * i + 32), l8 = *(const LAS float*)(lds + L_L + 48 * i + 32);
      const float M = fmaxf(fmaxf(fmaxf(fmaxf(m0[0], m0[1]), m0[2]), fmaxf(fmaxf(m0[3], m1[0]), m1[1])), fmaxf(fmaxf(m1[2], m1[3]), m8));
      float L = __builtin_amdgcn_exp2f(m8 - M) * l8;
#pragma unroll
      for (int w = 0; w < 4; ++w) { L = fmaf(__builtin_amdgcn_exp2f(m0[w] - M), l0[w], L); L = fmaf(__builtin_amdgcn_exp2f(m1[w] - M), l1[w], L); }
      lse = M + __builtin_amdgcn_logf(L); inv = __builtin_amdgcn_rcpf(L);
      PIN(lse); PIN(inv);
    }
    BARRIER();
    STAMP(6);
    u32x4 S[3];
#pragma unroll
    for (int s = 0; s < 3; ++s) S[s] = lds128(lds + L_QK + 1024 * s + 16 * lane);
#pragma unroll
    for (int h = 0; h < 2; ++h) {
      f32x4 T = z4;
#pragma unroll
      for (int s = 0; s < 3; ++s) T = mma32(W[h][s], S[s], T);
      const u32x4 gr = {pk(T[0] * inv, T[1] * inv), pk(T[2] * inv, T[3] * inv), __float_as_uint(lse), tag};
      __builtin_amdgcn_raw_buffer_store_b128(gr, rws, oaddr, 4 * INBOX * h, 16);
    }
    STAMP(7);
    if (DRAIN && b < NCONS) WAITVM(0);
  }
  if (b < NCONS) {
    const unsigned tag = *(const LAS unsigned*)(lds + L_FL + 16);
    const unsigned ia = b * INBOX + wave * 1024 + lane * 16;
    BARRIER();
    STAMP(8);
    if (wave < 8) {
      u32x4 g0 = __builtin_amdgcn_raw_buffer_load_b128(rws, ia, 0, 16), g1 = __builtin_amdgcn_raw_buffer_load_b128(rws, ia, 8192, 16);
      for (unsigned spin = 0; spin < (1u << 20); ++spin) {
        const bool m0 = g0[3] != tag, m1 = g1[3] != tag;
        if (!__any(m0 | m1)) break;
        asm volatile("" ::: "memory");
        if (m0) g0 = __builtin_amdgcn_raw_buffer_load_b128(rws, ia, 0, 16);
        if (m1) g1 = __builtin_amdgcn_raw_buffer_load_b128(rws, ia, 8192, 16);
      }
      const unsigned x0 = g0[0], x1 = g0[1], y0 = g1[0], y1 = g1[1], e0 = g0[2], e1 = g1[2];
      float mx = fmaxf(__uint_as_float(e0), __uint_as_float(e1));
      mx = fmaxf(mx, dpp<ROR4>(mx)); mx = fmaxf(mx, dpp<ROR8>(mx)); mx = xg_max(mx);
      const float w0 = __builtin_amdgcn_exp2f(__uint_as_float(e0) - mx), w1 = __builtin_amdgcn_exp2f(__uint_as_float(e1) - mx);
      float a[5] = {fmaf(w0, bflo(x0), w1 * bflo(y0)), fmaf(w0, bfhi(x0), w1 * bfhi(y0)), fmaf(w0, bflo(x1), w1 * bflo(y1)), fmaf(w0, bfhi(x1), w1 * bfhi(y1)), w0 + w1};
#pragma unroll
      for (int c = 0; c < 5; ++c) { a[c] += dpp<ROR4>(a[c]); a[c] += dpp<ROR8>(a[c]); a[c] = xg_sum(a[c]); }
      if (lane < 4) {
        *(LAS f32x4*)(lds + L_WA + 32 * (wave * 4 + lane)) = f32x4{a[0], a[1], a[2], a[3]};
        *(LAS f32x2*)(lds + L_WA + 32 * (wave * 4 + lane) + 16) = f32x2{a[4], mx};
      }
    }
    BARRIER();
    if (wave == 2) {
      const f32x4 v = ldsf4(lds + L_WA + 32 * (lane & 31));
      const f32x2 wm = *(const LAS f32x2*)(lds + L_WA + 32 * (lane & 31) + 16);
      float mx = wm[1];
      mx = fmaxf(mx, dpp<ROR4>(mx)); mx = fmaxf(mx, dpp<ROR8>(mx)); mx = xg_max(mx);
      const float w = __builtin_amdgcn_exp2f(wm[1] - mx);
      float a[5] = {w * v[0], w * v[1], w * v[2], w * v[3], w * wm[0]};
#pragma unroll
      for (int c = 0; c < 5; ++c) { a[c] += dpp<ROR4>(a[c]); a[c] += dpp<ROR8>(a[c]); a[c] = xg_sum(a[c]); }
      const float inv = __builtin_amdgcn_rcpf(a[4]);
      const unsigned q0 = qres[0], q1 = qres[1];
      if (lane < 4) *(GAS u32x2*)(out + (4 * (b / 24) + lane) * 96 + 4 * (b % 24)) =
          u32x2{pk(fmaf(a[0], inv, bflo(q0)), fmaf(a[1], inv, bfhi(q0))), pk(fmaf(a[2], inv, bflo(q1)), fmaf(a[3], inv, bfhi(q1)))};
      if (b == 0 && lane == 0) __builtin_amdgcn_raw_buffer_store_b32(tag, rws, 0, WS_EPOCH, 16);
    }
  }
  STAMP(9);
  STAMP_OUT();
}

__global__ void __launch_bounds__(64) k_pre(unsigned long long* p) { if (threadIdx.x == 0) *p = __builtin_amdgcn_s_memrealtime(); }
__global__ void __launch_bounds__(256) k_spin(const unsigned long long* ts, const unsigned long long* pre) {
  const unsigned tin = (unsigned)__builtin_amdgcn_s_memrealtime();
  __shared__ unsigned r[4][256];
  const int b = threadIdx.x;
  unsigned mn0 = ~0u, xm = 0;
  for (int w = 0; w < 12; ++w) { mn0 = min(mn0, (unsigned)ts[(b * 16 + w) * 10]); xm = max(xm, (unsigned)ts[(b * 16 + w) * 10 + 9]); }
  r[0][b] = mn0; r[1][b] = xm; __syncthreads();
  for (int o = 128; o > 0; o >>= 1) { if (b < o) { r[0][b] = min(r[0][b], r[0][b + o]); r[1][b] = max(r[1][b], r[1][b + o]); } __syncthreads(); }
  unsigned v[4] = {0, (r[0][0] - (unsigned)pre[1]) * 20, (tin - r[1][0]) * 20, ((unsigned)pre[1] - (unsigned)pre[0]) * 20};
  asm volatile("" : "+v"(v[0]), "+v"(v[1]), "+v"(v[2]), "+v"(v[3]));
  const unsigned long long until = __builtin_amdgcn_s_memrealtime() + v[ST];
  while (__builtin_amdgcn_s_memrealtime() < until) ;
}

extern "C" void kernel_launch(void* const* d_in, const int* in_sizes, int n_in, void* d_out, int out_size, void* d_ws, size_t ws_size,
                              hipStream_t stream) {
  unsigned long long* pre = (unsigned long long*)((char*)d_ws + (80u << 20));
  k_pre<<<1, 64, 0, stream>>>(pre);
  k_pre<<<1, 64, 0, stream>>>(pre + 1);
  k_main<<<NWG, 768, 0, stream>>>((const u16*)d_in[0], (const u16*)d_in[1], (const u16*)d_in[2], (const u16*)d_in[3], (const u16*)d_in[4],
                                  (const u16*)d_in[5], (const u16*)d_in[6], (u16*)d_out, (char*)d_ws);
  k_spin<<<1, 256, 0, stream>>>((const unsigned long long*)((char*)d_ws + (64u << 20)), pre);
}
```

```cpp
#define TOUCH
#define OUTSC1
#include <hip/hip_runtime.h>
#include <hip/hip_bf16.h>
#pragma clang diagnostic ignored "-Wint-to-pointer-cast"

typedef unsigned short u16;
typedef __attribute__((ext_vector_type(4))) short s16x4;
typedef __attribute__((ext_vector_type(8))) __bf16 bf16x8;
typedef __attribute__((ext_vector_type(2))) __bf16 bf16x2;
typedef __attribute__((ext_vector_type(2))) float f32x2;
typedef __attribute__((ext_vector_type(4))) float f32x4;
typedef __attribute__((ext_vector_type(4))) unsigned u32x4;
typedef __attribute__((ext_vector_type(2))) unsigned u32x2;
#define LAS __attribute__((address_space(3)))
#define GAS __attribute__((address_space(1)))
#define DEVI __device__ __forceinline__

#define PREWRITE 1
#define DRAIN 1
#define WPOL ""
#define WAUX 0
#define VSTART 4
#define CODEPF 8192
constexpr int NWG = 256;
constexpr int NH = 3;
constexpr int NSW = 9;
constexpr int NCONS = 96;
constexpr int REG = 15360;
constexpr int NLATE = 3;
constexpr int L_WQ = (NSW - NLATE) * REG;
constexpr int L_WK = L_WQ + 18432;
constexpr int L_Q = L_WK + 18432;
constexpr int L_WA = NSW * REG;
constexpr int L_QK = L_WA + 18432;
constexpr int L_M = L_QK + 3072;
constexpr int L_L = L_M + 768;
constexpr int L_FL = L_L + 768;
constexpr int L_END = L_FL + 32;
constexpr unsigned INBOX = 16384 + 256;
constexpr unsigned WS_EPOCH = NCONS * INBOX;
constexpr float EPS = 1e-5f;
constexpr float LOG2E = 1.4426950408889634f;
constexpr float THR = 40.f;

DEVI float bflo(unsigned x) { return __uint_as_float(x << 16); }
DEVI float bfhi(unsigned x) { return __uint_as_float(x & 0xffff0000u); }
DEVI unsigned pk(float a, float b) { f32x2 p = {a, b}; bf16x2 h = __builtin_convertvector(p, bf16x2); return __builtin_bit_cast(unsigned, h); }
DEVI f32x4 mma32(u32x4 a, u32x4 b, f32x4 c) { return __builtin_amdgcn_mfma_f32_16x16x32_bf16(__builtin_bit_cast(bf16x8, a), __builtin_bit_cast(bf16x8, b), c, 0, 0, 0); }
DEVI f32x4 mma4(float a, float b, f32x4 c) { return __builtin_amdgcn_mfma_f32_16x16x4f32(a, b, c, 0, 0, 0); }
DEVI float xg_sum(float x) {
  auto s = __builtin_amdgcn_permlane16_swap(__float_as_uint(x), __float_as_uint(x), false, false);
  x = __uint_as_float(s[0]) + __uint_as_float(s[1]);
  auto t = __builtin_amdgcn_permlane32_swap(__float_as_uint(x), __float_as_uint(x), false, false);
  return __uint_as_float(t[0]) + __uint_as_float(t[1]);
}
DEVI float xg_max(float x) {
  auto s = __builtin_amdgcn_permlane16_swap(__float_as_uint(x), __float_as_uint(x), false, false);
  x = fmaxf(__uint_as_float(s[0]), __uint_as_float(s[1]));
  auto t = __builtin_amdgcn_permlane32_swap(__float_as_uint(x), __float_as_uint(x), false, false);
  return fmaxf(__uint_as_float(t[0]), __uint_as_float(t[1]));
}
template <int CTRL> DEVI float dpp(float x) { return __builtin_bit_cast(float, __builtin_amdgcn_mov_dpp(__builtin_bit_cast(int, x), CTRL, 0xf, 0xf, true)); }
constexpr int ROR4 = 0x124, ROR8 = 0x128;
DEVI u32x4 lds128(unsigned a) { return *(const LAS u32x4*)a; }
DEVI f32x4 ldsf4(unsigned a) { return *(const LAS f32x4*)a; }
DEVI u32x2 ldstr(unsigned a) { s16x4 t = __builtin_amdgcn_ds_read_tr16_b64_v4i16((LAS s16x4*)a); return __builtin_bit_cast(u32x2, t); }
DEVI u32x4 ldstr2(unsigned a, unsigned c) { const u32x2 x = ldstr(a), y = ldstr(c); return u32x4{x[0], x[1], y[0], y[1]}; }
DEVI __amdgpu_buffer_rsrc_t rsrc(const void* p, unsigned bytes) { return __builtin_amdgcn_make_buffer_rsrc((void*)p, 0, bytes, 0x00020000); }
#define BARRIER() do { asm volatile("s_waitcnt lgkmcnt(0)" ::: "memory"); __builtin_amdgcn_s_barrier(); asm volatile("" ::: "memory"); } while (0)
#define WAITVM(n) asm volatile("s_waitcnt vmcnt(" #n ")" ::: "memory")
#define PIN(x) asm volatile("" : "+v"(x))
DEVI void post(unsigned a, int lane) {
  asm volatile("s_waitcnt lgkmcnt(0)" ::: "memory");
  if (lane == 0) __hip_atomic_fetch_add((LAS unsigned*)a, 1u, __ATOMIC_RELAXED, __HIP_MEMORY_SCOPE_WORKGROUP);
}
DEVI void await(unsigned a, unsigned n) {
  while (__builtin_amdgcn_readfirstlane(__hip_atomic_load((LAS unsigned*)a, __ATOMIC_RELAXED, __HIP_MEMORY_SCOPE_WORKGROUP)) < n) __builtin_amdgcn_s_sleep(1);
  asm volatile("" ::: "memory");
}
#define DMA3(POL, r, so, la, va, vb, vc) do { unsigned keep_; \
  asm volatile("s_mov_b32 %0, m0\n\ts_mov_b32 m0, %6\n\ts_nop 0\n\t" \
               "buffer_load_dwordx4 %1, %4, %5 offen " POL "lds\n\tbuffer_load_dwordx4 %2, %4, %5 offen offset:1024 " POL "lds\n\t" \
               "buffer_load_dwordx4 %3, %4, %5 offen offset:2048 " POL "lds\n\ts_mov_b32 m0, %0" \
               : "=&s"(keep_) : "v"(va), "v"(vb), "v"(vc), "s"(r), "s"(so), "s"(la) : "memory"); } while (0)

#define STAMP(k)
#define STAMP_DECL
#define STAMP_OUT()

__global__ void __launch_bounds__(768) k_main(const u16* __restrict__ q, const u16* __restrict__ f0, const u16* __restrict__ f1,
                                              const u16* __restrict__ wq, const u16* __restrict__ wk, const u16* __restrict__ wv,
                                              const u16* __restrict__ wo, u16* out, char* ws) {
  __shared__ __attribute__((aligned(16))) char lds_raw[L_END];
  const unsigned lds = (unsigned)(size_t)(LAS char*)lds_raw;
  const int lane = threadIdx.x & 63, i = lane & 15, g = lane >> 4;
  const int tq = (lane >> 2) & 3, tp = lane & 3;
  const int wave = __builtin_amdgcn_readfirstlane(threadIdx.x >> 6);
  const int b = blockIdx.x;
  const f32x4 z4 = {0.f, 0.f, 0.f, 0.f};
  STAMP_DECL; STAMP(0);
  if (threadIdx.x < 4) *(LAS unsigned*)(lds + L_FL + 4 * threadIdx.x) = 0u;
  BARRIER();
  const __amdgpu_buffer_rsrc_t rws = rsrc(ws, WS_EPOCH + 64);
  u32x2 qres;

  if (wave >= NH) {
    const int s = wave - NH;
    const unsigned base = lds + s * REG;
    const unsigned u = NSW * b + s, yp = (u * 43691u) >> 19;
    const unsigned o0 = u * 6144u + yp * 73728u;
    const unsigned v0 = (lane * 16) ^ (lane >= 48 ? 32 : 0), v1 = (lane * 16) ^ (lane < 32 ? 32 : 0), v2 = (lane * 16) ^ (lane >= 16 ? 32 : 0);
    const __amdgpu_buffer_rsrc_t r0 = rsrc(f0, 384u * 384u * 192u), r1 = rsrc(f1, 192u * 192u * 192u);
    if (s >= NSW - NLATE) await(lds + L_FL + 8, 1);
    DMA3("nt ", r1, u * 3072u, base, v0, v1, v2);
    DMA3("nt ", r0, o0, base + 3072, v0, v1, v2);
    DMA3("nt ", r0, o0 + 73728u, base + 6144, v0, v1, v2);
    DMA3("nt ", r0, o0 + 3072u, base + 9216, v0, v1, v2);
    DMA3("nt ", r0, o0 + 76800u, base + 12288, v0, v1, v2);
    const unsigned aoff = base + 192 * i + 16 * (g ^ (((i >> 2) & 1) << 1));
    const unsigned foff = base + 192 * (i >> 1) + 16 * (g ^ ((i >> 3) << 1));
    const unsigned tb = base + 192 * (4 * g + tq) + 8 * tp;
    const unsigned tE = tb + 32 * (g & 1), tO = tb - 32 * (g & 1);
    const unsigned fb = base + 192 * (2 * g + (tq >> 1)) + 8 * tp;
    const unsigned fE = fb + 32 * (g >> 1), fO = fb - 32 * (g >> 1);
    u32x4 ones = {0x3f803f80u, 0x3f803f80u, 0x3f803f80u, 0x3f803f80u};
    PIN(ones);
    const bool dg = g == (i >> 2);
    const float dmask = dg ? 1.f : 0.f, kneg = dg ? -1.f / 96.f : 0.f;
    const bool b0 = i & 1, b1 = i & 2;
    const float c0 = 96.f * EPS;
    STAMP(1);
    await(lds + L_FL + 8, 1);
    STAMP(2);
    u32x4 qk[3];
#pragma unroll
    for (int k = 0; k < 3; ++k) qk[k] = lds128(lds + L_QK + 1024 * k + 16 * lane);
    f32x4 acc[6];
#pragma unroll
    for (int t = 0; t < 6; ++t) acc[t] = z4;
    float m = 0.f, l = 0.f;
#pragma unroll
    for (int h = 0; h < 2; ++h) {
      if (h == 0) WAITVM(9); else WAITVM(3);
      if (h == 0) STAMP(3);
      u32x4 F[3], At[6];
#pragma unroll
      for (int k = 0; k < 3; ++k) F[k] = lds128(foff + 1536 * h + 64 * k);
#pragma unroll
      for (int t = 0; t < 6; ++t) { const u32x2 y = ldstr(((t & 1) ? fO : fE) + 1536 * h + 32 * t); At[t][2] = y[0]; At[t][3] = y[1]; }
      f32x4 d1 = z4, s1f = z4, gff = {c0, c0, c0, c0};
#pragma unroll
      for (int dy = 0; dy < 2; ++dy) {
        if (dy == 1) { if (h == 0) WAITVM(6); else WAITVM(0); }
        if (dy == 1 && h == 1) STAMP(4);
        const int slot = 3072 * (1 + dy + 2 * h);
        u32x4 X[3];
#pragma unroll
        for (int k = 0; k < 3; ++k) X[k] = lds128(aoff + slot + 64 * k);
#pragma unroll
        for (int t = 0; t < 6; ++t) { const u32x2 y = ldstr(((t & 1) ? tO : tE) + slot + 32 * t); At[t][0] = y[0]; At[t][1] = y[1]; }
        if (dy == 0) {
#pragma unroll
          for (int k = 0; k < 3; ++k) { gff = mma32(F[k], F[k], gff); s1f = mma32(ones, F[k], s1f); d1 = mma32(F[k], qk[k], d1); }
        }
        f32x4 gxx = gff, gxf = z4, s1 = s1f, dl = d1;
#pragma unroll
        for (int k = 0; k < 3; ++k) { gxx = mma32(X[k], X[k], gxx); gxf = mma32(X[k], F[k], gxf); s1 = mma32(ones, X[k], s1); }
#pragma unroll
        for (int k = 0; k < 3; ++k) dl = mma32(X[k], qk[k], dl);
        const float u0 = fmaf(2.f, gxf[0], gxx[0]), u1 = fmaf(2.f, gxf[1], gxx[1]), u2 = fmaf(2.f, gxf[2], gxx[2]), u3 = fmaf(2.f, gxf[3], gxx[3]);
        const float ua = b0 ? u1 : u0, ub = b0 ? u3 : u2, ss = b1 ? ub : ua;
        const float rsm = __builtin_amdgcn_rsqf(fmaxf(fmaf(s1[0] * kneg, s1[0], ss), c0)) * dmask;
        const f32x4 rs = mma4(rsm, 1.f, z4);
        float a[4];
        if (h == 0 && dy == 0) {
#pragma unroll
          for (int r = 0; r < 4; ++r) a[r] = dl[r] * rs[r];
          m = xg_max(fmaxf(fmaxf(a[0], a[1]), fmaxf(a[2], a[3])));
#pragma unroll
          for (int r = 0; r < 4; ++r) a[r] -= m;
        } else {
#pragma unroll
          for (int r = 0; r < 4; ++r) a[r] = fmaf(dl[r], rs[r], -m);
          const float mx = fmaxf(fmaxf(a[0], a[1]), fmaxf(a[2], a[3]));
          if (__builtin_expect(__any(mx > THR), 0)) {
            const float d = fmaxf(xg_max(mx), 0.f), al = __builtin_amdgcn_exp2f(-d);
            m += d; l *= al;
#pragma unroll
            for (int t = 0; t < 6; ++t) acc[t] *= al;
#pragma unroll
            for (int r = 0; r < 4; ++r) a[r] -= d;
          }
        }
        float pp[4];
#pragma unroll
        for (int r = 0; r < 4; ++r) { const float p = __builtin_amdgcn_exp2f(a[r]); l += p; pp[r] = p * rs[r]; }
        const unsigned pa = pk(pp[0], pp[1]), pb = pk(pp[2], pp[3]);
        const u32x4 P = {pa, pb, pa, pb};
#pragma unroll
        for (int t = 0; t < 6; ++t) acc[t] = mma32(At[t], P, acc[t]);
        if (!(h == 1 && dy == 1)) {
#pragma unroll
          for (int t = 0; t < 6; ++t) PIN(acc[t]);
          PIN(l);
        }
      }
    }
    l = xg_sum(l);
#pragma unroll
    for (int t = 0; t < 6; ++t) *(LAS f32x4*)(base + 1024 * t + 16 * lane) = acc[t];
    if (g == 0) { *(LAS float*)(lds + L_M + 48 * i + 4 * s) = m; *(LAS float*)(lds + L_L + 48 * i + 4 * s) = l; }
    STAMP(5);
    BARRIER();
    STAMP(6);
    if (s < 6) {
      const f32x4 m0 = ldsf4(lds + L_M + 48 * i), m1 = ldsf4(lds + L_M + 48 * i + 16);
      const float m8 = *(const LAS float*)(lds + L_M + 48 * i + 32);
      f32x4 pw[NSW];
#pragma unroll
      for (int w = 0; w < NSW; ++w) pw[w] = ldsf4(lds + w * REG + 1024 * s + 16 * lane);
      const float M = fmaxf(fmaxf(fmaxf(fmaxf(m0[0], m0[1]), m0[2]), fmaxf(fmaxf(m0[3], m1[0]), m1[1])), fmaxf(fmaxf(m1[2], m1[3]), m8));
      f32x4 S = __builtin_amdgcn_exp2f(m8 - M) * pw[8];
#pragma unroll
      for (int w = 0; w < 4; ++w) { S += __builtin_amdgcn_exp2f(m0[w] - M) * pw[w]; S += __builtin_amdgcn_exp2f(m1[w] - M) * pw[4 + w]; }
      *(LAS u32x2*)(lds + L_QK + 1024 * (s >> 1) + 16 * lane + 8 * (s & 1)) = u32x2{pk(S[0], S[1]), pk(S[2], S[3])};
    }
    BARRIER();
    STAMP(7);
  } else {
    const int k = wave;
    const unsigned vl = lane * 16;
    if (k < 2) {
      const __amdgpu_buffer_rsrc_t rq = rsrc(q, 3072), rwq = rsrc(wq, 18432), rwk = rsrc(wk, 18432);
      const unsigned so = 9216 * k;
      if (k == 0) DMA3(WPOL, rq, 0, lds + L_Q, vl, vl, vl);
#pragma unroll
      for (int j = 0; j < 3; ++j) DMA3(WPOL, rwq, so + 3072 * j, lds + L_WQ + so + 3072 * j, vl, vl, vl);
#pragma unroll
      for (int j = 0; j < 3; ++j) DMA3(WPOL, rwk, so + 3072 * j, lds + L_WK + so + 3072 * j, vl, vl, vl);
      STAMP(1);
      if (k == 1) {
        WAITVM(9); post(lds + L_FL, lane);
        WAITVM(0); post(lds + L_FL + 4, lane);
      } else {
        WAITVM(18);
        u32x4 qn[3];
        {
          float x[24], sm = 0.f;
#pragma unroll
          for (int s = 0; s < 3; ++s) {
            const u32x4 w4 = lds128(lds + L_Q + 192 * i + 64 * s + 16 * g);
#pragma unroll
            for (int j = 0; j < 4; ++j) { const unsigned w = w4[j]; x[8 * s + 2 * j] = bflo(w); x[8 * s + 2 * j + 1] = bfhi(w); sm += bflo(w) + bfhi(w); }
          }
          const float mean = xg_sum(sm) * (1.f / 96.f);
          float vs = 0.f;
#pragma unroll
          for (int j = 0; j < 24; ++j) { x[j] -= mean; vs = fmaf(x[j], x[j], vs); }
          const float rstd = __builtin_amdgcn_rsqf(xg_sum(vs) * (1.f / 96.f) + EPS);
#pragma unroll
          for (int j = 0; j < 12; ++j) qn[j >> 2][j & 3] = pk(x[2 * j] * rstd, x[2 * j + 1] * rstd);
#pragma unroll
          for (int s = 0; s < 3; ++s) PIN(qn[s]);
        }
        WAITVM(9);
        await(lds + L_FL, 1);
        STAMP(2);
        u32x4 Qb[3];
        const unsigned ta = lds + L_WQ + 192 * (8 * g + tq) + 16 * tp;
#pragma unroll
        for (int kk = 0; kk < 3; ++kk) {
          f32x4 T[2] = {z4, z4};
#pragma unroll
          for (int s = 0; s < 3; ++s)
#pragma unroll
            for (int h = 0; h < 2; ++h) T[h] = mma32(ldstr2(ta + 6144 * s + 64 * kk + 8 * h, ta + 6144 * s + 768 + 64 * kk + 8 * h), qn[s], T[h]);
          Qb[kk] = u32x4{pk(T[0][0], T[0][1]), pk(T[0][2], T[0][3]), pk(T[1][0], T[1][1]), pk(T[1][2], T[1][3])};
          PIN(Qb[kk]);
        }
        WAITVM(0);
        await(lds + L_FL + 4, 1);
        STAMP(3);
        const unsigned wa = lds + L_WK + 192 * (8 * (i >> 2) + (i & 3)) + 16 * g;
        f32x4 D[6]; float cs = 0.f;
#pragma unroll
        for (int t = 0; t < 6; ++t) {
          D[t] = z4;
#pragma unroll
          for (int kk = 0; kk < 3; ++kk) D[t] = mma32(lds128(wa + 6144 * (t >> 1) + 768 * (t & 1) + 64 * kk), Qb[kk], D[t]);
          cs += (D[t][0] + D[t][1]) + (D[t][2] + D[t][3]);
        }
        const float mu = xg_sum(cs) * (LOG2E / 96.f);
#pragma unroll
        for (int s = 0; s < 3; ++s)
          *(LAS u32x4*)(lds + L_QK + 1024 * s + 16 * lane) =
              u32x4{pk(fmaf(D[2 * s][0], LOG2E, -mu), fmaf(D[2 * s][1], LOG2E, -mu)), pk(fmaf(D[2 * s][2], LOG2E, -mu), fmaf(D[2 * s][3], LOG2E, -mu)),
                    pk(fmaf(D[2 * s + 1][0], LOG2E, -mu), fmaf(D[2 * s + 1][1], LOG2E, -mu)), pk(fmaf(D[2 * s + 1][2], LOG2E, -mu), fmaf(D[2 * s + 1][3], LOG2E, -mu))};
        post(lds + L_FL + 8, lane);
      }
    } else {
      unsigned pf = 0;
      if (CODEPF && lane < 4) {
        const unsigned off = (b >> 3) * 512 + lane * 128;
        if (off < CODEPF) pf = *(const GAS unsigned*)((__builtin_amdgcn_s_getpc() & ~0xffull) + off);
      }
      {
        const char* tp_ = lane == 0 ? (const char*)wv : lane == 1 ? (const char*)wo : lane == 2 ? (const char*)out : (const char*)ws + (lane - 3) * 262144;
        if (lane < 10) pf ^= *(const GAS unsigned*)tp_;
      }
      unsigned epoch = __builtin_amdgcn_raw_buffer_load_b32(rws, 0, WS_EPOCH, 16);
      const int c = b < NCONS ? b : 0;
      qres = *(const GAS u32x2*)(q + (4 * (c / 24) + tp) * 96 + 4 * (c % 24));
      PIN(epoch); PIN(qres); PIN(pf);
      if (lane == 0) *(LAS unsigned*)(lds + L_FL + 16) = epoch + 1u;
    }
    if (k == 2) await(lds + L_FL + VSTART, 1);
    STAMP(4);
    u32x4 Bt[2][3];
    {
      const __amdgpu_buffer_rsrc_t rwv = rsrc(wv, 18432), rwo = rsrc(wo, 18432);
      const unsigned so = 6144 * k;
#pragma unroll
      for (int j = 0; j < 2; ++j) DMA3(WPOL, rwv, so + 3072 * j, lds + L_WA + so + 3072 * j, vl, vl, vl);
      const unsigned rowo = 192 * (8 * (i >> 2) + (i & 3)) + 16 * g;
      u32x4 w[6];
#pragma unroll
      for (int j = 0; j < 6; ++j) w[j] = __builtin_amdgcn_raw_buffer_load_b128(rwo, rowo + 768 * (j & 1), 6144 * (j >> 1) + 64 * k, WAUX);
      const unsigned one = (i & 1) ? 0x3f800000u : 0x00003f80u;
#pragma unroll
      for (int h = 0; h < 2; ++h) {
        u32x4 id;
#pragma unroll
        for (int e = 0; e < 4; ++e) id[e] = (g == 2 * h + (i >> 3) && ((i & 7) >> 1) == e) ? one : 0u;
#pragma unroll
        for (int s = 0; s < 3; ++s) {
          const f32x4 a = mma32(w[2 * s], id, z4), c = mma32(w[2 * s + 1], id, z4);
          Bt[h][s] = u32x4{pk(a[0], a[1]), pk(a[2], a[3]), pk(c[0], c[1]), pk(c[2], c[3])};
          PIN(Bt[h][s]);
        }
      }
    }
    WAITVM(0);
    post(lds + L_FL + 12, lane);
    await(lds + L_FL + 12, NH);
    const unsigned tag = *(const LAS unsigned*)(lds + L_FL + 16);
    const unsigned oaddr = (((i >> 2) * 3 + k) * 8 + g) * INBOX + b * 64 + (i & 3) * 16;
    if (PREWRITE) {
      __builtin_amdgcn_raw_buffer_store_b128(u32x4{0u, 0u, 0u, tag - 1u}, rws, oaddr, 0, 16);
      __builtin_amdgcn_raw_buffer_store_b128(u32x4{0u, 0u, 0u, tag - 1u}, rws, oaddr, 4 * INBOX, 16);
    }
    u32x4 W[2][3];
    {
      f32x4 D[2][6];
#pragma unroll
      for (int t = 0; t < 6; ++t) {
        D[0][t] = z4; D[1][t] = z4;
#pragma unroll
        for (int s = 0; s < 3; ++s) {
          const u32x4 A = lds128(lds + L_WA + 3072 * t + 192 * i + 64 * s + 16 * g);
          D[0][t] = mma32(A, Bt[0][s], D[0][t]); D[1][t] = mma32(A, Bt[1][s], D[1][t]);
        }
      }
#pragma unroll
      for (int h = 0; h < 2; ++h) {
        float cs = 0.f;
#pragma unroll
        for (int t = 0; t < 6; ++t) cs += (D[h][t][0] + D[h][t][1]) + (D[h][t][2] + D[h][t][3]);
        const float sc = 9.797958971132712f, mu = xg_sum(cs) * (sc / 96.f);
#pragma unroll
        for (int t = 0; t < 6; ++t) {
          W[h][t >> 1][2 * (t & 1)] = pk(fmaf(D[h][t][0], sc, -mu), fmaf(D[h][t][1], sc, -mu));
          W[h][t >> 1][2 * (t & 1) + 1] = pk(fmaf(D[h][t][2], sc, -mu), fmaf(D[h][t][3], sc, -mu));
        }
#pragma unroll
        for (int s = 0; s < 3; ++s) PIN(W[h][s]);
      }
    }
    WAITVM(0);
    STAMP(5);
    BARRIER();
    float lse, inv;
    {
      const f32x4 m0 = ldsf4(lds + L_M + 48 * i), m1 = ldsf4(lds + L_M + 48 * i + 16), l0 = ldsf4(lds + L_L + 48 * i), l1 = ldsf4(lds + L_L + 48 * i + 16);
      const float m8 = *(const LAS float*)(lds + L_M + 48 * i + 32), l8 = *(const LAS float*)(lds + L_L + 48 * i + 32);
      const float M = fmaxf(fmaxf(fmaxf(fmaxf(m0[0], m0[1]), m0[2]), fmaxf(fmaxf(m0[3], m1[0]), m1[1])), fmaxf(fmaxf(m1[2], m1[3]), m8));
      float L = __builtin_amdgcn_exp2f(m8 - M) * l8;
#pragma unroll
      for (int w = 0; w < 4; ++w) { L = fmaf(__builtin_amdgcn_exp2f(m0[w] - M), l0[w], L); L = fmaf(__builtin_amdgcn_exp2f(m1[w] - M), l1[w], L); }
      lse = M + __builtin_amdgcn_logf(L); inv = __builtin_amdgcn_rcpf(L);
      PIN(lse); PIN(inv);
    }
    BARRIER();
    STAMP(6);
    u32x4 S[3];
#pragma unroll
    for (int s = 0; s < 3; ++s) S[s] = lds128(lds + L_QK + 1024 * s + 16 * lane);
#pragma unroll
    for (int h = 0; h < 2; ++h) {
      f32x4 T = z4;
#pragma unroll
      for (int s = 0; s < 3; ++s) T = mma32(W[h][s], S[s], T);
      const u32x4 gr = {pk(T[0] * inv, T[1] * inv), pk(T[2] * inv, T[3] * inv), __float_as_uint(lse), tag};
      __builtin_amdgcn_raw_buffer_store_b128(gr, rws, oaddr, 4 * INBOX * h, 16);
    }
    STAMP(7);
    if (DRAIN && b < NCONS) WAITVM(0);
  }
  if (b < NCONS) {
    const unsigned tag = *(const LAS unsigned*)(lds + L_FL + 16);
    const unsigned ia = b * INBOX + wave * 1024 + lane * 16;
    BARRIER();
    STAMP(8);
    if (wave < 8) {
      u32x4 g0 = __builtin_amdgcn_raw_buffer_load_b128(rws, ia, 0, 16), g1 = __builtin_amdgcn_raw_buffer_load_b128(rws, ia, 8192, 16);
      for (unsigned spin = 0; spin < (1u << 20); ++spin) {
        const bool m0 = g0[3] != tag, m1 = g1[3] != tag;
        if (!__any(m0 | m1)) break;
        asm volatile("" ::: "memory");
        if (m0) g0 = __builtin_amdgcn_raw_buffer_load_b128(rws, ia, 0, 16);
        if (m1) g1 = __builtin_amdgcn_raw_buffer_load_b128(rws, ia, 8192, 16);
      }
      const unsigned x0 = g0[0], x1 = g0[1], y0 = g1[0], y1 = g1[1], e0 = g0[2], e1 = g1[2];
      float mx = fmaxf(__uint_as_float(e0), __uint_as_float(e1));
      mx = fmaxf(mx, dpp<ROR4>(mx)); mx = fmaxf(mx, dpp<ROR8>(mx)); mx = xg_max(mx);
      const float w0 = __builtin_amdgcn_exp2f(__uint_as_float(e0) - mx), w1 = __builtin_amdgcn_exp2f(__uint_as_float(e1) - mx);
      float a[5] = {fmaf(w0, bflo(x0), w1 * bflo(y0)), fmaf(w0, bfhi(x0), w1 * bfhi(y0)), fmaf(w0, bflo(x1), w1 * bflo(y1)), fmaf(w0, bfhi(x1), w1 * bfhi(y1)), w0 + w1};
#pragma unroll
      for (int c = 0; c < 5; ++c) { a[c] += dpp<ROR4>(a[c]); a[c] += dpp<ROR8>(a[c]); a[c] = xg_sum(a[c]); }
      if (lane < 4) {
        *(LAS f32x4*)(lds + L_WA + 32 * (wave * 4 + lane)) = f32x4{a[0], a[1], a[2], a[3]};
        *(LAS f32x2*)(lds + L_WA + 32 * (wave * 4 + lane) + 16) = f32x2{a[4], mx};
      }
    }
    BARRIER();
    if (wave == 2) {
      const f32x4 v = ldsf4(lds + L_WA + 32 * (lane & 31));
      const f32x2 wm = *(const LAS f32x2*)(lds + L_WA + 32 * (lane & 31) + 16);
      float mx = wm[1];
      mx = fmaxf(mx, dpp<ROR4>(mx)); mx = fmaxf(mx, dpp<ROR8>(mx)); mx = xg_max(mx);
      const float w = __builtin_amdgcn_exp2f(wm[1] - mx);
      float a[5] = {w * v[0], w * v[1], w * v[2], w * v[3], w * wm[0]};
#pragma unroll
      for (int c = 0; c < 5; ++c) { a[c] += dpp<ROR4>(a[c]); a[c] += dpp<ROR8>(a[c]); a[c] = xg_sum(a[c]); }
      const float inv = __builtin_amdgcn_rcpf(a[4]);
      const unsigned q0 = qres[0], q1 = qres[1];
      const u32x2 ov = {pk(fmaf(a[0], inv, bflo(q0)), fmaf(a[1], inv, bfhi(q0))), pk(fmaf(a[2], inv, bflo(q1)), fmaf(a[3], inv, bfhi(q1)))};
      if (lane < 4) __builtin_amdgcn_raw_buffer_store_b64(ov, rsrc(out, 3072), lane * 192, (b / 24) * 768 + (b % 24) * 8, 16);
      if (b == 0 && lane == 0) __builtin_amdgcn_raw_buffer_store_b32(tag, rws, 0, WS_EPOCH, 16);
    }
  }
  STAMP(9);
  STAMP_OUT();
}

extern "C" void kernel_launch(void* const* d_in, const int* in_sizes, int n_in, void* d_out, int out_size, void* d_ws, size_t ws_size,
                              hipStream_t stream) {
  k_main<<<NWG, 768, 0, stream>>>((const u16*)d_in[0], (const u16*)d_in[1], (const u16*)d_in[2], (const u16*)d_in[3], (const u16*)d_in[4],
                                  (const u16*)d_in[5], (const u16*)d_in[6], (u16*)d_out, (char*)d_ws);
}
```

```cpp
#include <hip/hip_runtime.h>
#include <hip/hip_bf16.h>
#include <type_traits>
#pragma clang diagnostic ignored "-Wint-to-pointer-cast"

typedef unsigned short u16;
typedef unsigned long long u64;
typedef __attribute__((ext_vector_type(4))) short s16x4;
typedef __attribute__((ext_vector_type(8))) __bf16 bf16x8;
typedef __attribute__((ext_vector_type(2))) __bf16 bf16x2;
typedef __attribute__((ext_vector_type(2))) float f32x2;
typedef __attribute__((ext_vector_type(4))) float f32x4;
typedef __attribute__((ext_vector_type(4))) unsigned u32x4;
typedef __attribute__((ext_vector_type(2))) unsigned u32x2;
typedef __attribute__((ext_vector_type(16))) unsigned u32x16;
#define LAS __attribute__((address_space(3)))
#define GAS __attribute__((address_space(1)))
#define DEVI __device__ __forceinline__

#define VEARLY 1
#define VSLEEP 6
#define HMODE 2
#define HTH 64
#define HX 760
#define HSL 12
#define DRAIN 1
constexpr int NWG = 256;
constexpr int NCONS = 96;
constexpr int BLK = 15360;
constexpr int slot(int k, int h) { return k * BLK + 3072 + 6144 * h; }
constexpr int L_SP = 9 * BLK;
constexpr int bandq(int j) { return j < 2 ? L_SP + 6144 * j : slot(7 + ((j - 2) >> 1), j & 1); }
constexpr int bandv(int j) { return j < 2 ? L_SP + 6144 * j : slot(1 + ((j - 2) >> 1), j & 1); }
constexpr int L_Q = L_SP + 12288;
constexpr int L_QK = L_SP + 15360;
constexpr int L_V = L_QK + 3072;
constexpr int L_E = L_V + 3072;
constexpr int L_M = L_E + 512;
constexpr int L_L = L_M + 256;
constexpr int L_Z = L_L + 256;
constexpr int L_LSE = L_Z + 256;
constexpr int L_C = L_LSE + 64;
constexpr int L_FL = L_C + 768;
constexpr int L_MR = L_FL + 128;
constexpr int L_END = L_MR + 64;
enum { F_P = 0, F_ALL = 4, F_QK = 8, F_OV = 12, F_DEV = 16, F_TAG = 20, F_V = 24, F_DONE = 32  , F_R1 = 48  , F_W = 64  , F_GO = 72 };
static_assert(BLK == 15360 && L_QK == 153600 && L_E == 159744 && L_FL == 161856 && L_MR == 161984 && F_P == 0 && F_ALL == 4 && F_QK == 8 && F_DEV == 16 && F_DONE == 32 && F_R1 == 48 && F_W == 64 && L_SP == 138240 && L_C == 161088 && L_V == 156672 && L_M == 160256 && L_LSE == 161024 && F_V == 24 && F_TAG == 20 && F_OV == 12, "constants of the assembly blocks");
constexpr unsigned INBOX = 16384 + 256;
constexpr unsigned WS_EPOCH = NCONS * INBOX;
constexpr float EPS = 1e-5f;
constexpr float LOG2E = 1.4426950408889634f;
constexpr float THR = 80.f;

DEVI float bflo(unsigned x) { return __uint_as_float(x << 16); }
DEVI float bfhi(unsigned x) { return __uint_as_float(x & 0xffff0000u); }
DEVI unsigned pk(float a, float b) { f32x2 p = {a, b}; bf16x2 h = __builtin_convertvector(p, bf16x2); return __builtin_bit_cast(unsigned, h); }
DEVI f32x4 mma32(u32x4 a, u32x4 b, f32x4 c) { return __builtin_amdgcn_mfma_f32_16x16x32_bf16(__builtin_bit_cast(bf16x8, a), __builtin_bit_cast(bf16x8, b), c, 0, 0, 0); }
DEVI float sw16(float a, float b) { auto s = __builtin_amdgcn_permlane16_swap(__float_as_uint(a), __float_as_uint(b), false, false); return __uint_as_float(s[0]) + __uint_as_float(s[1]); }
DEVI float sw32(float a, float b) { auto s = __builtin_amdgcn_permlane32_swap(__float_as_uint(a), __float_as_uint(b), false, false); return __uint_as_float(s[0]) + __uint_as_float(s[1]); }
DEVI float xg_sum(float x) { x = sw16(x, x); return sw32(x, x); }
DEVI float xg_max(float x) {
  auto s = __builtin_amdgcn_permlane16_swap(__float_as_uint(x), __float_as_uint(x), false, false);
  x = fmaxf(__uint_as_float(s[0]), __uint_as_float(s[1]));
  auto t = __builtin_amdgcn_permlane32_swap(__float_as_uint(x), __float_as_uint(x), false, false);
  return fmaxf(__uint_as_float(t[0]), __uint_as_float(t[1]));
}
template <int CTRL> DEVI float dpp(float x) { return __builtin_bit_cast(float, __builtin_amdgcn_mov_dpp(__builtin_bit_cast(int, x), CTRL, 0xf, 0xf, true)); }
constexpr int ROR4 = 0x124, ROR8 = 0x128;
DEVI float sel(u64 mask, float t, float f) { float r; asm("v_cndmask_b32 %0, %1, %2, %3" : "=v"(r) : "v"(f), "v"(t), "s"(mask)); return r; }
DEVI u32x4 lds128(unsigned a) { return *(const LAS u32x4*)a; }
DEVI f32x4 ldsf4(unsigned a) { return *(const LAS f32x4*)a; }
DEVI float ldsf(unsigned a) { return *(const LAS float*)a; }
DEVI unsigned ldsu(unsigned a) { return *(const LAS unsigned*)a; }
DEVI u32x2 ldstr(unsigned a) { s16x4 t = __builtin_amdgcn_ds_read_tr16_b64_v4i16((LAS s16x4*)a); return __builtin_bit_cast(u32x2, t); }
DEVI u32x4 ldstr2(unsigned a, unsigned c) { const u32x2 x = ldstr(a), y = ldstr(c); return u32x4{x[0], x[1], y[0], y[1]}; }
DEVI u64 mk64(unsigned lo, unsigned hi) { return (u64)lo | ((u64)hi << 32); }
DEVI __amdgpu_buffer_rsrc_t rsrc(u64 p, unsigned bytes) { return __builtin_amdgcn_make_buffer_rsrc((void*)p, 0, bytes, 0x00020000); }
#define BARRIER() asm volatile("s_waitcnt lgkmcnt(0)\n\ts_barrier" ::: "memory")
#define WAITVM(n) asm volatile("s_waitcnt vmcnt(" #n ")" ::: "memory")
#define PIN(x) asm volatile("" : "+v"(x))
#define PINS(x) asm volatile("" : "+s"(x))
#define SB() __builtin_amdgcn_sched_barrier(0)
#define MM0(d, a, b) asm volatile("v_mfma_f32_16x16x32_bf16 %0, %1, %2, 0" : "=&v"(d) : "v"(a), "v"(b))
#define MMC(d, a, b, c) asm volatile("v_mfma_f32_16x16x32_bf16 %0, %1, %2, %3" : "=&v"(d) : "v"(a), "v"(b), "v"(c))
#define MMA(d, a, b) asm volatile("v_mfma_f32_16x16x32_bf16 %0, %1, %2, %0" : "+v"(d) : "v"(a), "v"(b))
#define MM3(d, a, b, C, c) asm volatile("v_mfma_f32_16x16x32_bf16 %0, %1, %4, " C "\n\tv_mfma_f32_16x16x32_bf16 %0, %2, %5, %0\n\tv_mfma_f32_16x16x32_bf16 %0, %3, %6, %0" \
                                        : "=&v"(d) : "v"(a[0]), "v"(a[1]), "v"(a[2]), "v"(b[0]), "v"(b[1]), "v"(b[2]), "v"(c))
template <int N> using IC = std::integral_constant<int, N>;
#define NOP1(n, x) asm volatile("s_nop " #n : "+v"(x))
#define NOP2(n, x, y) asm volatile("s_nop " #n : "+v"(x), "+v"(y))
DEVI void post(unsigned a) {
  asm volatile("ds_write_b32 %0, %1" :: "v"(a), "v"(1u) : "memory");
}
DEVI unsigned await_ne(unsigned a, unsigned unset) {
  unsigned r;
  asm volatile("L%=:\n\tds_read_b32 %0, %1\n\ts_waitcnt lgkmcnt(0)\n\tv_cmp_eq_u32 vcc, %2, %0\n\ts_cbranch_vccnz L%=" : "=&v"(r) : "v"(a), "s"(unset) : "memory", "vcc");
  return r;
}
DEVI void await_ge(unsigned a, int n) {
  unsigned r;
  asm volatile("L%=:\n\tds_read_b32 %0, %1\n\ts_waitcnt lgkmcnt(0)\n\tv_cmp_gt_i32 vcc, %2, %0\n\ts_cbranch_vccnz L%=" : "=&v"(r) : "v"(a), "s"(n) : "memory", "vcc");
}
DEVI void await(unsigned a) {
  unsigned r;
  asm volatile("s_branch M%=\n\tL%=:\n\ts_sleep 1\n\tM%=:\n\tds_read_b32 %0, %1\n\ts_waitcnt lgkmcnt(0)\n\tv_cmp_eq_u32 vcc, 0, %0\n\ts_cbranch_vccnz L%=" : "=&v"(r) : "v"(a) : "memory", "vcc");
}
#define DMA3(POL, r, so, la, va, vb, vc) \
  asm volatile("s_mov_b32 m0, %5\n\ts_nop 0\n\t" \
               "buffer_load_dwordx4 %0, %3, %4 offen " POL "lds\n\tbuffer_load_dwordx4 %1, %3, %4 offen offset:1024 " POL "lds\n\t" \
               "buffer_load_dwordx4 %2, %3, %4 offen offset:2048 " POL "lds" \
               :: "v"(va), "v"(vb), "v"(vc), "s"(r), "s"(so), "s"(la) : "memory")

#define STAMP(k)
#define STAMP_DECL
#define STAMP_OUT()

__global__ void __launch_bounds__(640) k_main(const u16* a0, const u16* a1, const u16* a2, const u16* a3, const u16* a4, const u16* a5,
                                              const u16* a6, u16* a7, char* a8) {
  __shared__ __attribute__((aligned(16))) char lds_raw[L_END];
  const unsigned lds = (unsigned)(size_t)(LAS char*)lds_raw;
  unsigned tid = threadIdx.x;
#define LANES() PIN(tid); const int lane = tid & 63, i = lane & 15, g = lane >> 4, tq = (lane >> 2) & 3, tp = lane & 3; const unsigned vl = lane * 16; \
                (void)i; (void)g; (void)tq; (void)tp; (void)vl
  const int b = blockIdx.x;
  const f32x4 z4 = {0.f, 0.f, 0.f, 0.f};
  STAMP_DECL; STAMP(0);
  u32x16 ka; u32x2 kb; int wave; u64 t_in;
  asm volatile(
      "s_load_dwordx16 s[36:51], %[kp], 0x0\n\t"
      "s_load_dwordx2 s[52:53], %[kp], 0x40\n\t"
      "s_memrealtime s[34:35]\n\t"
      "v_readfirstlane_b32 s54, %[tid]\n\t"
      "v_and_b32 v7, 63, %[tid]\n\t"
      "v_mov_b32 v8, 161856\n\t"
      "s_lshr_b32 s54, s54, 6\n\t"
      "v_lshlrev_b32 v2, 4, v7\n\t"
      "s_cmp_lg_u32 s54, 9\n\t"
      "s_cbranch_scc1 NI%=\n\t"
      "v_cmp_lt_u32 vcc, 31, v7\n\t"
      "v_min_u32 v6, 47, v7\n\t"
      "v_lshl_add_u32 v6, v6, 2, v8\n\t"
      "v_cndmask_b32 v3, 0, -1, vcc\n\t"
      "ds_write_b32 v6, v3\n\t"
      "s_waitcnt lgkmcnt(0)\n\t"
      "NI%=:\n\t"
      "s_cmp_lg_u32 s54, 1\n\t"
      "s_cbranch_scc1 NP%=\n\t"
      "s_getpc_b64 s[60:61]\n\t"
      "s_lshr_b32 s68, %[b], 3\n\t"
      "s_lshl_b32 s68, s68, 9\n\t"
      "v_and_b32 v6, 3, v7\n\t"
      "s_and_b32 s60, s60, 0xffffff00\n\t"
      "v_lshl_add_u32 v6, v6, 7, s68\n\t"
      "s_mov_b32 m0, 161088\n\t"
      "v_min_u32 v6, 13696, v6\n\t"
      "global_load_lds_dword v6, s[60:61]\n\t"
      "s_mul_i32 s69, %[b], 9\n\t"
      "s_mul_i32 s71, s69, 3072\n\t"
      "s_mul_i32 s68, s69, 43691\n\t"
      "s_lshr_b32 s68, s68, 19\n\t"
      "s_mul_i32 s68, s68, 73728\n\t"
      "s_lshl_b32 s70, s71, 1\n\t"
      "s_add_u32 s70, s70, s68\n\t"
      "s_mov_b32 s72, 0\n\t"
      "s_mov_b32 s73, 0xffff0000\n\t"
      "s_mov_b32 s74, -1\n\t"
      "s_mov_b32 s75, 0\n\t"
      "s_mov_b32 s76, 0xffff0000\n\t"
      "s_mov_b32 s77, -1\n\t"
      "v_xor_b32 v6, 32, v2\n\t"
      "v_cndmask_b32 v3, v2, v6, s[72:73]\n\t"
      "v_cndmask_b32 v4, v2, v6, s[74:75]\n\t"
      "v_cndmask_b32 v5, v2, v6, s[76:77]\n\t"
      "v_mov_b32 v6, -5\n\t"
      "s_mov_b32 s58, 0\n\t"
      "s_mov_b32 s62, 0x1b00000\n\t"
      "s_mov_b32 s63, 0x20000\n\t"
      "s_mov_b32 s66, 0x6c0000\n\t"
      "s_mov_b32 s67, 0x20000\n\t"
      "NP%=:\n\t"
      "s_barrier\n\t"
      "s_cmp_lg_u32 s54, 1\n\t"
      "s_waitcnt lgkmcnt(0)\n\t"
      "s_cbranch_scc1 NL%=\n\t"
      "s_mov_b32 s64, s38\n\t"
      "s_and_b32 s65, s39, 0xffff\n\t"
      "s_mov_b32 s60, s36\n\t"
      "s_and_b32 s61, s37, 0xffff\n\t"
      "s_add_u32 m0, s58, 0\n\t"
      "s_add_u32 s68, s71, 0\n\t"
      "buffer_load_dwordx4 v3, s[64:67], s68 offen offset:0 nt lds\n\t"
      "buffer_load_dwordx4 v4, s[64:67], s68 offen offset:1024 nt lds\n\t"
      "buffer_load_dwordx4 v5, s[64:67], s68 offen offset:2048 nt lds\n\t"
      "s_add_u32 m0, s58, 3072\n\t"
      "s_add_u32 s68, s70, 0\n\t"
      "buffer_load_dwordx4 v3, s[60:63], s68 offen offset:0 nt lds\n\t"
      "buffer_load_dwordx4 v4, s[60:63], s68 offen offset:1024 nt lds\n\t"
      "buffer_load_dwordx4 v5, s[60:63], s68 offen offset:2048 nt lds\n\t"
      "s_add_u32 m0, s58, 9216\n\t"
      "s_add_u32 s68, s70, 3072\n\t"
      "buffer_load_dwordx4 v3, s[60:63], s68 offen offset:0 nt lds\n\t"
      "buffer_load_dwordx4 v4, s[60:63], s68 offen offset:1024 nt lds\n\t"
      "buffer_load_dwordx4 v5, s[60:63], s68 offen offset:2048 nt lds\n\t"
      "s_add_u32 m0, s58, 6144\n\t"
      "s_add_u32 s68, s70, 73728\n\t"
      "buffer_load_dwordx4 v3, s[60:63], s68 offen offset:0 nt lds\n\t"
      "buffer_load_dwordx4 v4, s[60:63], s68 offen offset:1024 nt lds\n\t"
      "buffer_load_dwordx4 v5, s[60:63], s68 offen offset:2048 nt lds\n\t"
      "s_add_u32 m0, s58, 12288\n\t"
      "s_add_u32 s68, s70, 76800\n\t"
      "buffer_load_dwordx4 v3, s[60:63], s68 offen offset:0 nt lds\n\t"
      "buffer_load_dwordx4 v4, s[60:63], s68 offen offset:1024 nt lds\n\t"
      "buffer_load_dwordx4 v5, s[60:63], s68 offen offset:2048 nt lds\n\t"
      "s_branch LD%=\n\t"
      "NL%=:\n\t"
      "s_cmp_gt_u32 s54, 3\n\t"
      "s_cbranch_scc1 TCH%=\n\t"
      "s_cmp_lg_u32 s54, 0\n\t"
      "s_cbranch_scc1 WT%=\n\t"
      "s_mov_b32 s60, s40\n\t"
      "s_and_b32 s61, s41, 0xffff\n\t"
      "s_mov_b32 s62, 3072\n\t"
      "s_mov_b32 s63, 0x20000\n\t"
      "s_mov_b32 m0, 150528\n\t"
      "s_nop 0\n\t"
      "buffer_load_dwordx4 v2, s[60:63], 0 offen offset:0 lds\n\t"
      "buffer_load_dwordx4 v2, s[60:63], 0 offen offset:1024 lds\n\t"
      "buffer_load_dwordx4 v2, s[60:63], 0 offen offset:2048 lds\n\t"
      "s_branch END%=\n\t"
      "WT%=:\n\t"
      "s_and_b32 s69, s54, 1\n\t"
      "s_mul_i32 s58, s69, 3072\n\t"
      "s_mov_b32 s60, s42\n\t"
      "s_and_b32 s61, s43, 0xffff\n\t"
      "s_mov_b32 s62, 18432\n\t"
      "s_mov_b32 s63, 0x20000\n\t"
      "s_add_u32 m0, s58, 138240\n\t"
      "s_add_u32 s68, s58, 0\n\t"
      "buffer_load_dwordx4 v2, s[60:63], s68 offen offset:0 lds\n\t"
      "buffer_load_dwordx4 v2, s[60:63], s68 offen offset:1024 lds\n\t"
      "buffer_load_dwordx4 v2, s[60:63], s68 offen offset:2048 lds\n\t"
      "s_add_u32 m0, s58, 144384\n\t"
      "s_add_u32 s68, s58, 6144\n\t"
      "buffer_load_dwordx4 v2, s[60:63], s68 offen offset:0 lds\n\t"
      "buffer_load_dwordx4 v2, s[60:63], s68 offen offset:1024 lds\n\t"
      "buffer_load_dwordx4 v2, s[60:63], s68 offen offset:2048 lds\n\t"
      "s_add_u32 m0, s58, 110592\n\t"
      "s_add_u32 s68, s58, 12288\n\t"
      "buffer_load_dwordx4 v2, s[60:63], s68 offen offset:0 lds\n\t"
      "buffer_load_dwordx4 v2, s[60:63], s68 offen offset:1024 lds\n\t"
      "buffer_load_dwordx4 v2, s[60:63], s68 offen offset:2048 lds\n\t"
      "s_mov_b32 s60, s44\n\t"
      "s_and_b32 s61, s45, 0xffff\n\t"
      "s_add_u32 m0, s58, 116736\n\t"
      "s_add_u32 s68, s58, 0\n\t"
      "buffer_load_dwordx4 v2, s[60:63], s68 offen offset:0 lds\n\t"
      "buffer_load_dwordx4 v2, s[60:63], s68 offen offset:1024 lds\n\t"
      "buffer_load_dwordx4 v2, s[60:63], s68 offen offset:2048 lds\n\t"
      "s_add_u32 m0, s58, 125952\n\t"
      "s_add_u32 s68, s58, 6144\n\t"
      "buffer_load_dwordx4 v2, s[60:63], s68 offen offset:0 lds\n\t"
      "buffer_load_dwordx4 v2, s[60:63], s68 offen offset:1024 lds\n\t"
      "buffer_load_dwordx4 v2, s[60:63], s68 offen offset:2048 lds\n\t"
      "s_add_u32 m0, s58, 132096\n\t"
      "s_add_u32 s68, s58, 12288\n\t"
      "buffer_load_dwordx4 v2, s[60:63], s68 offen offset:0 lds\n\t"
      "buffer_load_dwordx4 v2, s[60:63], s68 offen offset:1024 lds\n\t"
      "buffer_load_dwordx4 v2, s[60:63], s68 offen offset:2048 lds\n\t"
      "s_lshl_b32 s69, s69, 2\n\t"
      "v_add_u32 v8, s69, v8\n\t"
      "v_mov_b32 v6, 1\n\t"
      "s_waitcnt vmcnt(15)\n\t"
      "ds_write_b32 v8, v6 offset:64\n\t"
      "v_mov_b32 v6, 2\n\t"
      "s_waitcnt vmcnt(12)\n\t"
      "ds_write_b32 v8, v6 offset:64\n\t"
      "v_mov_b32 v6, 3\n\t"
      "s_waitcnt vmcnt(9)\n\t"
      "ds_write_b32 v8, v6 offset:64\n\t"
      "v_mov_b32 v6, 4\n\t"
      "s_waitcnt vmcnt(6)\n\t"
      "ds_write_b32 v8, v6 offset:64\n\t"
      "v_mov_b32 v6, 5\n\t"
      "s_waitcnt vmcnt(3)\n\t"
      "ds_write_b32 v8, v6 offset:64\n\t"
      "v_mov_b32 v6, 6\n\t"
      "s_waitcnt vmcnt(0)\n\t"
      "ds_write_b32 v8, v6 offset:64\n\t"
      "s_branch END%=\n\t"
      "TCH%=:\n\t"
      "s_cmp_gt_u32 s54, 7\n\t"
      "s_cbranch_scc1 TS%=\n\t"
      "s_sub_u32 s69, s54, 4\n\t"
      "s_lshl_b32 s69, s69, 1\n\t"
      "s_mov_b32 m0, s69\n\t"
      "s_cmp_lt_u32 s54, 6\n\t"
      "s_movrels_b64 s[60:61], s[46:47]\n\t"
      "s_cselect_b32 s68, 4, 0\n\t"
      "s_cselect_b32 s69, 12, 18\n\t"
      "s_cmp_eq_u32 s54, 7\n\t"
      "s_cselect_b32 s68, 6, s68\n\t"
      "v_min_u32 v6, s68, v7\n\t"
      "s_mov_b32 m0, 161344\n\t"
      "v_lshlrev_b32 v6, s69, v6\n\t"
      "global_load_lds_dword v6, s[60:61]\n\t"
      "s_branch END%=\n\t"
      "TS%=:\n\t"
      "s_mul_i32 s69, %[b], 9\n\t"
      "s_mov_b32 m0, 161344\n\t"
      "s_cmp_eq_u32 s54, 9\n\t"
      "s_cbranch_scc1 TF%=\n\t"
      "s_mov_b32 s60, s36\n\t"
      "s_and_b32 s61, s37, 0xffff\n\t"
      "s_mov_b32 s62, 28311552\n\t"
      "s_mov_b32 s63, 0x20000\n\t"
      "v_lshrrev_b32 v6, 2, v7\n\t"
      "v_min_u32 v6, 8, v6\n\t"
      "v_add_u32 v6, s69, v6\n\t"
      "v_mul_u32_u24 v3, 43691, v6\n\t"
      "v_lshrrev_b32 v3, 19, v3\n\t"
      "v_bfe_u32 v4, v7, 1, 1\n\t"
      "v_add_u32 v3, v3, v4\n\t"
      "v_mul_u32_u24 v3, 73728, v3\n\t"
      "v_mul_u32_u24 v6, 6144, v6\n\t"
      "v_and_b32 v4, 1, v7\n\t"
      "v_lshlrev_b32 v4, 12, v4\n\t"
      "v_add3_u32 v6, v6, v3, v4\n\t"
      "buffer_load_dword v6, s[60:63], 0 offen lds\n\t"
      "s_branch END%=\n\t"
      "TF%=:\n\t"
      "s_mov_b32 s60, s38\n\t"
      "s_and_b32 s61, s39, 0xffff\n\t"
      "s_mov_b32 s62, 7077888\n\t"
      "s_mov_b32 s63, 0x20000\n\t"
      "s_mul_i32 s69, s69, 3072\n\t"
      "v_min_u32 v6, 6, v7\n\t"
      "v_lshlrev_b32 v6, 12, v6\n\t"
      "buffer_load_dword v6, s[60:63], s69 offen lds\n\t"
      "s_branch END%=\n\t"
      "LD%=:\n\t"
      "s_add_u32 s69, s69, 1\n\t"
      "s_add_u32 s57, s71, 3072\n\t"
      "s_mov_b32 s58, 15360\n\t"
      "s_mov_b32 s59, 2\n\t"
      "RND%=:\n\t"
      "s_mul_i32 s68, s69, 43691\n\t"
      "s_lshr_b32 s68, s68, 19\n\t"
      "s_mul_i32 s68, s68, 73728\n\t"
      "s_mul_i32 s55, s69, 6144\n\t"
      "s_add_u32 s55, s55, s68\n\t"
      "s_add_u32 s69, s69, 1\n\t"
      "s_add_u32 m0, s58, 0\n\t"
      "s_add_u32 s68, s57, 0\n\t"
      "buffer_load_dwordx4 v3, s[64:67], s68 offen offset:0 nt lds\n\t"
      "buffer_load_dwordx4 v4, s[64:67], s68 offen offset:1024 nt lds\n\t"
      "buffer_load_dwordx4 v5, s[64:67], s68 offen offset:2048 nt lds\n\t"
      "s_waitcnt vmcnt(30)\n\t"
      "v_add_u32 v6, 1, v6\n\t"
      "ds_write_b32 v8, v6\n\t"
      "s_mul_i32 s68, s69, 43691\n\t"
      "s_lshr_b32 s68, s68, 19\n\t"
      "s_mul_i32 s68, s68, 73728\n\t"
      "s_mul_i32 s56, s69, 6144\n\t"
      "s_add_u32 s56, s56, s68\n\t"
      "s_add_u32 s69, s69, 1\n\t"
      "s_add_u32 m0, s58, 15360\n\t"
      "s_add_u32 s68, s57, 3072\n\t"
      "buffer_load_dwordx4 v3, s[64:67], s68 offen offset:0 nt lds\n\t"
      "buffer_load_dwordx4 v4, s[64:67], s68 offen offset:1024 nt lds\n\t"
      "buffer_load_dwordx4 v5, s[64:67], s68 offen offset:2048 nt lds\n\t"
      "s_waitcnt vmcnt(30)\n\t"
      "v_add_u32 v6, 1, v6\n\t"
      "ds_write_b32 v8, v6\n\t"
      "s_add_u32 m0, s58, 3072\n\t"
      "s_add_u32 s68, s55, 0\n\t"
      "buffer_load_dwordx4 v3, s[60:63], s68 offen offset:0 nt lds\n\t"
      "buffer_load_dwordx4 v4, s[60:63], s68 offen offset:1024 nt lds\n\t"
      "buffer_load_dwordx4 v5, s[60:63], s68 offen offset:2048 nt lds\n\t"
      "s_waitcnt vmcnt(30)\n\t"
      "v_add_u32 v6, 1, v6\n\t"
      "ds_write_b32 v8, v6\n\t"
      "s_add_u32 m0, s58, 9216\n\t"
      "s_add_u32 s68, s55, 3072\n\t"
      "buffer_load_dwordx4 v3, s[60:63], s68 offen offset:0 nt lds\n\t"
      "buffer_load_dwordx4 v4, s[60:63], s68 offen offset:1024 nt lds\n\t"
      "buffer_load_dwordx4 v5, s[60:63], s68 offen offset:2048 nt lds\n\t"
      "s_waitcnt vmcnt(30)\n\t"
      "v_add_u32 v6, 1, v6\n\t"
      "ds_write_b32 v8, v6\n\t"
      "s_add_u32 m0, s58, 18432\n\t"
      "s_add_u32 s68, s56, 0\n\t"
      "buffer_load_dwordx4 v3, s[60:63], s68 offen offset:0 nt lds\n\t"
      "buffer_load_dwordx4 v4, s[60:63], s68 offen offset:1024 nt lds\n\t"
      "buffer_load_dwordx4 v5, s[60:63], s68 offen offset:2048 nt lds\n\t"
      "s_waitcnt vmcnt(30)\n\t"
      "v_add_u32 v6, 1, v6\n\t"
      "ds_write_b32 v8, v6\n\t"
      "s_add_u32 m0, s58, 24576\n\t"
      "s_add_u32 s68, s56, 3072\n\t"
      "buffer_load_dwordx4 v3, s[60:63], s68 offen offset:0 nt lds\n\t"
      "buffer_load_dwordx4 v4, s[60:63], s68 offen offset:1024 nt lds\n\t"
      "buffer_load_dwordx4 v5, s[60:63], s68 offen offset:2048 nt lds\n\t"
      "s_waitcnt vmcnt(30)\n\t"
      "v_add_u32 v6, 1, v6\n\t"
      "ds_write_b32 v8, v6\n\t"
      "s_add_u32 m0, s58, 6144\n\t"
      "s_add_u32 s68, s55, 73728\n\t"
      "buffer_load_dwordx4 v3, s[60:63], s68 offen offset:0 nt lds\n\t"
      "buffer_load_dwordx4 v4, s[60:63], s68 offen offset:1024 nt lds\n\t"
      "buffer_load_dwordx4 v5, s[60:63], s68 offen offset:2048 nt lds\n\t"
      "s_waitcnt vmcnt(30)\n\t"
      "v_add_u32 v6, 1, v6\n\t"
      "ds_write_b32 v8, v6\n\t"
      "s_add_u32 m0, s58, 12288\n\t"
      "s_add_u32 s68, s55, 76800\n\t"
      "buffer_load_dwordx4 v3, s[60:63], s68 offen offset:0 nt lds\n\t"
      "buffer_load_dwordx4 v4, s[60:63], s68 offen offset:1024 nt lds\n\t"
      "buffer_load_dwordx4 v5, s[60:63], s68 offen offset:2048 nt lds\n\t"
      "s_waitcnt vmcnt(30)\n\t"
      "v_add_u32 v6, 1, v6\n\t"
      "ds_write_b32 v8, v6\n\t"
      "s_add_u32 m0, s58, 21504\n\t"
      "s_add_u32 s68, s56, 73728\n\t"
      "buffer_load_dwordx4 v3, s[60:63], s68 offen offset:0 nt lds\n\t"
      "buffer_load_dwordx4 v4, s[60:63], s68 offen offset:1024 nt lds\n\t"
      "buffer_load_dwordx4 v5, s[60:63], s68 offen offset:2048 nt lds\n\t"
      "s_waitcnt vmcnt(30)\n\t"
      "v_add_u32 v6, 1, v6\n\t"
      "ds_write_b32 v8, v6\n\t"
      "s_add_u32 m0, s58, 27648\n\t"
      "s_add_u32 s68, s56, 76800\n\t"
      "buffer_load_dwordx4 v3, s[60:63], s68 offen offset:0 nt lds\n\t"
      "buffer_load_dwordx4 v4, s[60:63], s68 offen offset:1024 nt lds\n\t"
      "buffer_load_dwordx4 v5, s[60:63], s68 offen offset:2048 nt lds\n\t"
      "s_waitcnt vmcnt(30)\n\t"
      "v_add_u32 v6, 1, v6\n\t"
      "ds_write_b32 v8, v6\n\t"
      "s_add_u32 s57, s57, 6144\n\t"
      "s_add_u32 s58, s58, 30720\n\t"
      "s_sub_u32 s59, s59, 1\n\t"
      "s_cmp_lg_u32 s59, 0\n\t"
      "s_cbranch_scc1 RND%=\n\t"
      "s_mul_i32 s68, s69, 43691\n\t"
      "s_lshr_b32 s68, s68, 19\n\t"
      "s_mul_i32 s68, s68, 73728\n\t"
      "s_mul_i32 s55, s69, 6144\n\t"
      "s_add_u32 s55, s55, s68\n\t"
      "s_add_u32 s69, s69, 1\n\t"
      "s_add_u32 m0, s58, 0\n\t"
      "s_add_u32 s68, s57, 0\n\t"
      "buffer_load_dwordx4 v3, s[64:67], s68 offen offset:0 nt lds\n\t"
      "buffer_load_dwordx4 v4, s[64:67], s68 offen offset:1024 nt lds\n\t"
      "buffer_load_dwordx4 v5, s[64:67], s68 offen offset:2048 nt lds\n\t"
      "s_waitcnt vmcnt(30)\n\t"
      "v_add_u32 v6, 1, v6\n\t"
      "ds_write_b32 v8, v6\n\t"
      "s_mul_i32 s68, s69, 43691\n\t"
      "s_lshr_b32 s68, s68, 19\n\t"
      "s_mul_i32 s68, s68, 73728\n\t"
      "s_mul_i32 s56, s69, 6144\n\t"
      "s_add_u32 s56, s56, s68\n\t"
      "s_add_u32 s69, s69, 1\n\t"
      "s_add_u32 m0, s58, 15360\n\t"
      "s_add_u32 s68, s57, 3072\n\t"
      "buffer_load_dwordx4 v3, s[64:67], s68 offen offset:0 nt lds\n\t"
      "buffer_load_dwordx4 v4, s[64:67], s68 offen offset:1024 nt lds\n\t"
      "buffer_load_dwordx4 v5, s[64:67], s68 offen offset:2048 nt lds\n\t"
      "s_waitcnt vmcnt(30)\n\t"
      "v_add_u32 v6, 1, v6\n\t"
      "ds_write_b32 v8, v6\n\t"
      "s_mul_i32 s68, s69, 43691\n\t"
      "s_lshr_b32 s68, s68, 19\n\t"
      "s_mul_i32 s68, s68, 73728\n\t"
      "s_mul_i32 s72, s69, 6144\n\t"
      "s_add_u32 s72, s72, s68\n\t"
      "s_add_u32 s69, s69, 1\n\t"
      "s_add_u32 m0, s58, 30720\n\t"
      "s_add_u32 s68, s57, 6144\n\t"
      "buffer_load_dwordx4 v3, s[64:67], s68 offen offset:0 nt lds\n\t"
      "buffer_load_dwordx4 v4, s[64:67], s68 offen offset:1024 nt lds\n\t"
      "buffer_load_dwordx4 v5, s[64:67], s68 offen offset:2048 nt lds\n\t"
      "s_waitcnt vmcnt(30)\n\t"
      "v_add_u32 v6, 1, v6\n\t"
      "ds_write_b32 v8, v6\n\t"
      "s_mul_i32 s68, s69, 43691\n\t"
      "s_lshr_b32 s68, s68, 19\n\t"
      "s_mul_i32 s68, s68, 73728\n\t"
      "s_mul_i32 s73, s69, 6144\n\t"
      "s_add_u32 s73, s73, s68\n\t"
      "s_add_u32 s69, s69, 1\n\t"
      "s_add_u32 m0, s58, 46080\n\t"
      "s_add_u32 s68, s57, 9216\n\t"
      "buffer_load_dwordx4 v3, s[64:67], s68 offen offset:0 nt lds\n\t"
      "buffer_load_dwordx4 v4, s[64:67], s68 offen offset:1024 nt lds\n\t"
      "buffer_load_dwordx4 v5, s[64:67], s68 offen offset:2048 nt lds\n\t"
      "s_waitcnt vmcnt(30)\n\t"
      "v_add_u32 v6, 1, v6\n\t"
      "ds_write_b32 v8, v6\n\t"
      "s_add_u32 m0, s58, 3072\n\t"
      "s_add_u32 s68, s55, 0\n\t"
      "buffer_load_dwordx4 v3, s[60:63], s68 offen offset:0 nt lds\n\t"
      "buffer_load_dwordx4 v4, s[60:63], s68 offen offset:1024 nt lds\n\t"
      "buffer_load_dwordx4 v5, s[60:63], s68 offen offset:2048 nt lds\n\t"
      "s_waitcnt vmcnt(30)\n\t"
      "v_add_u32 v6, 1, v6\n\t"
      "ds_write_b32 v8, v6\n\t"
      "s_add_u32 m0, s58, 9216\n\t"
      "s_add_u32 s68, s55, 3072\n\t"
      "buffer_load_dwordx4 v3, s[60:63], s68 offen offset:0 nt lds\n\t"
      "buffer_load_dwordx4 v4, s[60:63], s68 offen offset:1024 nt lds\n\t"
      "buffer_load_dwordx4 v5, s[60:63], s68 offen offset:2048 nt lds\n\t"
      "s_waitcnt vmcnt(30)\n\t"
      "v_add_u32 v6, 1, v6\n\t"
      "ds_write_b32 v8, v6\n\t"
      "s_add_u32 m0, s58, 18432\n\t"
      "s_add_u32 s68, s56, 0\n\t"
      "buffer_load_dwordx4 v3, s[60:63], s68 offen offset:0 nt lds\n\t"
      "buffer_load_dwordx4 v4, s[60:63], s68 offen offset:1024 nt lds\n\t"
      "buffer_load_dwordx4 v5, s[60:63], s68 offen offset:2048 nt lds\n\t"
      "s_waitcnt vmcnt(30)\n\t"
      "v_add_u32 v6, 1, v6\n\t"
      "ds_write_b32 v8, v6\n\t"
      "s_add_u32 m0, s58, 24576\n\t"
      "s_add_u32 s68, s56, 3072\n\t"
      "buffer_load_dwordx4 v3, s[60:63], s68 offen offset:0 nt lds\n\t"
      "buffer_load_dwordx4 v4, s[60:63], s68 offen offset:1024 nt lds\n\t"
      "buffer_load_dwordx4 v5, s[60:63], s68 offen offset:2048 nt lds\n\t"
      "s_waitcnt vmcnt(30)\n\t"
      "v_add_u32 v6, 1, v6\n\t"
      "ds_write_b32 v8, v6\n\t"
      "QW%=:\n\t"
      "ds_read_b32 v7, v8 offset:8\n\t"
      "s_waitcnt lgkmcnt(0)\n\t"
      "v_cmp_eq_u32 vcc, 0, v7\n\t"
      "s_cbranch_vccnz QW%=\n\t"
      "s_add_u32 m0, s58, 33792\n\t"
      "s_add_u32 s68, s72, 0\n\t"
      "buffer_load_dwordx4 v3, s[60:63], s68 offen offset:0 nt lds\n\t"
      "buffer_load_dwordx4 v4, s[60:63], s68 offen offset:1024 nt lds\n\t"
      "buffer_load_dwordx4 v5, s[60:63], s68 offen offset:2048 nt lds\n\t"
      "s_waitcnt vmcnt(30)\n\t"
      "v_add_u32 v6, 1, v6\n\t"
      "ds_write_b32 v8, v6\n\t"
      "s_add_u32 m0, s58, 39936\n\t"
      "s_add_u32 s68, s72, 3072\n\t"
      "buffer_load_dwordx4 v3, s[60:63], s68 offen offset:0 nt lds\n\t"
      "buffer_load_dwordx4 v4, s[60:63], s68 offen offset:1024 nt lds\n\t"
      "buffer_load_dwordx4 v5, s[60:63], s68 offen offset:2048 nt lds\n\t"
      "s_waitcnt vmcnt(30)\n\t"
      "v_add_u32 v6, 1, v6\n\t"
      "ds_write_b32 v8, v6\n\t"
      "s_add_u32 m0, s58, 49152\n\t"
      "s_add_u32 s68, s73, 0\n\t"
      "buffer_load_dwordx4 v3, s[60:63], s68 offen offset:0 nt lds\n\t"
      "buffer_load_dwordx4 v4, s[60:63], s68 offen offset:1024 nt lds\n\t"
      "buffer_load_dwordx4 v5, s[60:63], s68 offen offset:2048 nt lds\n\t"
      "s_waitcnt vmcnt(30)\n\t"
      "v_add_u32 v6, 1, v6\n\t"
      "ds_write_b32 v8, v6\n\t"
      "s_add_u32 m0, s58, 55296\n\t"
      "s_add_u32 s68, s73, 3072\n\t"
      "buffer_load_dwordx4 v3, s[60:63], s68 offen offset:0 nt lds\n\t"
      "buffer_load_dwordx4 v4, s[60:63], s68 offen offset:1024 nt lds\n\t"
      "buffer_load_dwordx4 v5, s[60:63], s68 offen offset:2048 nt lds\n\t"
      "s_waitcnt vmcnt(30)\n\t"
      "v_add_u32 v6, 1, v6\n\t"
      "ds_write_b32 v8, v6\n\t"
      "s_add_u32 m0, s58, 6144\n\t"
      "s_add_u32 s68, s55, 73728\n\t"
      "buffer_load_dwordx4 v3, s[60:63], s68 offen offset:0 nt lds\n\t"
      "buffer_load_dwordx4 v4, s[60:63], s68 offen offset:1024 nt lds\n\t"
      "buffer_load_dwordx4 v5, s[60:63], s68 offen offset:2048 nt lds\n\t"
      "s_waitcnt vmcnt(30)\n\t"
      "v_add_u32 v6, 1, v6\n\t"
      "ds_write_b32 v8, v6\n\t"
      "s_add_u32 m0, s58, 12288\n\t"
      "s_add_u32 s68, s55, 76800\n\t"
      "buffer_load_dwordx4 v3, s[60:63], s68 offen offset:0 nt lds\n\t"
      "buffer_load_dwordx4 v4, s[60:63], s68 offen offset:1024 nt lds\n\t"
      "buffer_load_dwordx4 v5, s[60:63], s68 offen offset:2048 nt lds\n\t"
      "s_waitcnt vmcnt(30)\n\t"
      "v_add_u32 v6, 1, v6\n\t"
      "ds_write_b32 v8, v6\n\t"
      "s_add_u32 m0, s58, 21504\n\t"
      "s_add_u32 s68, s56, 73728\n\t"
      "buffer_load_dwordx4 v3, s[60:63], s68 offen offset:0 nt lds\n\t"
      "buffer_load_dwordx4 v4, s[60:63], s68 offen offset:1024 nt lds\n\t"
      "buffer_load_dwordx4 v5, s[60:63], s68 offen offset:2048 nt lds\n\t"
      "s_waitcnt vmcnt(30)\n\t"
      "v_add_u32 v6, 1, v6\n\t"
      "ds_write_b32 v8, v6\n\t"
      "s_add_u32 m0, s58, 27648\n\t"
      "s_add_u32 s68, s56, 76800\n\t"
      "buffer_load_dwordx4 v3, s[60:63], s68 offen offset:0 nt lds\n\t"
      "buffer_load_dwordx4 v4, s[60:63], s68 offen offset:1024 nt lds\n\t"
      "buffer_load_dwordx4 v5, s[60:63], s68 offen offset:2048 nt lds\n\t"
      "s_waitcnt vmcnt(30)\n\t"
      "v_add_u32 v6, 1, v6\n\t"
      "ds_write_b32 v8, v6\n\t"
      "s_add_u32 m0, s58, 36864\n\t"
      "s_add_u32 s68, s72, 73728\n\t"
      "buffer_load_dwordx4 v3, s[60:63], s68 offen offset:0 nt lds\n\t"
      "buffer_load_dwordx4 v4, s[60:63], s68 offen offset:1024 nt lds\n\t"
      "buffer_load_dwordx4 v5, s[60:63], s68 offen offset:2048 nt lds\n\t"
      "s_waitcnt vmcnt(30)\n\t"
      "v_add_u32 v6, 1, v6\n\t"
      "ds_write_b32 v8, v6\n\t"
      "s_add_u32 m0, s58, 43008\n\t"
      "s_add_u32 s68, s72, 76800\n\t"
      "buffer_load_dwordx4 v3, s[60:63], s68 offen offset:0 nt lds\n\t"
      "buffer_load_dwordx4 v4, s[60:63], s68 offen offset:1024 nt lds\n\t"
      "buffer_load_dwordx4 v5, s[60:63], s68 offen offset:2048 nt lds\n\t"
      "s_waitcnt vmcnt(30)\n\t"
      "v_add_u32 v6, 1, v6\n\t"
      "ds_write_b32 v8, v6\n\t"
      "s_add_u32 m0, s58, 52224\n\t"
      "s_add_u32 s68, s73, 73728\n\t"
      "buffer_load_dwordx4 v3, s[60:63], s68 offen offset:0 nt lds\n\t"
      "buffer_load_dwordx4 v4, s[60:63], s68 offen offset:1024 nt lds\n\t"
      "buffer_load_dwordx4 v5, s[60:63], s68 offen offset:2048 nt lds\n\t"
      "s_waitcnt vmcnt(30)\n\t"
      "v_add_u32 v6, 1, v6\n\t"
      "ds_write_b32 v8, v6\n\t"
      "s_add_u32 m0, s58, 58368\n\t"
      "s_add_u32 s68, s73, 76800\n\t"
      "buffer_load_dwordx4 v3, s[60:63], s68 offen offset:0 nt lds\n\t"
      "buffer_load_dwordx4 v4, s[60:63], s68 offen offset:1024 nt lds\n\t"
      "buffer_load_dwordx4 v5, s[60:63], s68 offen offset:2048 nt lds\n\t"
      "s_waitcnt vmcnt(30)\n\t"
      "v_add_u32 v6, 1, v6\n\t"
      "ds_write_b32 v8, v6\n\t"
      "v_mov_b32 v7, 1\n\t"
      "ds_write_b32 v8, v7 offset:4\n\t"
      "v_add_u32 v6, 1, v6\n\t"
      "s_waitcnt vmcnt(27)\n\t"
      "ds_write_b32 v8, v6\n\t"
      "v_add_u32 v6, 1, v6\n\t"
      "s_waitcnt vmcnt(24)\n\t"
      "ds_write_b32 v8, v6\n\t"
      "v_add_u32 v6, 1, v6\n\t"
      "s_waitcnt vmcnt(21)\n\t"
      "ds_write_b32 v8, v6\n\t"
      "v_add_u32 v6, 1, v6\n\t"
      "s_waitcnt vmcnt(18)\n\t"
      "ds_write_b32 v8, v6\n\t"
      "v_add_u32 v6, 1, v6\n\t"
      "s_waitcnt vmcnt(15)\n\t"
      "ds_write_b32 v8, v6\n\t"
      "v_add_u32 v6, 1, v6\n\t"
      "s_waitcnt vmcnt(12)\n\t"
      "ds_write_b32 v8, v6\n\t"
      "v_add_u32 v6, 1, v6\n\t"
      "s_waitcnt vmcnt(9)\n\t"
      "ds_write_b32 v8, v6\n\t"
      "v_add_u32 v6, 1, v6\n\t"
      "s_waitcnt vmcnt(6)\n\t"
      "ds_write_b32 v8, v6\n\t"
      "v_add_u32 v6, 1, v6\n\t"
      "s_waitcnt vmcnt(3)\n\t"
      "ds_write_b32 v8, v6\n\t"
      "v_add_u32 v6, 1, v6\n\t"
      "s_waitcnt vmcnt(0)\n\t"
      "ds_write_b32 v8, v6\n\t"
      "END%=:\n\t"
      : "=&{s[36:51]}"(ka), "=&{s[52:53]}"(kb), "=&{s54}"(wave), "=&{s[34:35]}"(t_in) : [kp] "s"(__builtin_amdgcn_kernarg_segment_ptr()), [b] "s"(b), [tid] "v"(tid) : "memory", "vcc", "scc", "s55", "s56", "s57", "s58", "s59", "s60", "s61", "s62", "s63", "s64", "s65", "s66", "s67", "s68", "s69", "s70", "s71", "s72", "s73", "s74", "s75", "s76", "s77", "v2", "v3", "v4", "v5", "v6", "v7", "v8", "v9", "v10", "v11", "v12", "v13");
#define p_ws mk64(kb[0], kb[1])
#define rws rsrc(p_ws, WS_EPOCH + 64)
  u32x4 ones = {0x3f803f80u, 0x3f803f80u, 0x3f803f80u, 0x3f803f80u};
  unsigned qres = 0;
  if (wave == 0) {
    LANES();
    {
      unsigned ta = lds + 192 * (8 * g + tq) + 16 * tp, wa = lds + bandq(3) + 192 * (8 * (i >> 2) + (i & 3)) + 16 * g, qa = lds + L_QK + vl;
      unsigned ta0 = ta + bandq(0), ta2 = ta + bandq(2), f3 = lds + L_FL + F_QK, fw = lds + L_FL + F_W + 4 * (lane & 1);
      PIN(fw); PIN(ta0); PIN(ta2); PIN(wa); PIN(qa); PIN(f3);
      WAITVM(0);
      u32x4 qn[3];
      float rstd;
      {
        u32x4 w4[3];
#pragma unroll
        for (int s = 0; s < 3; ++s) w4[s] = lds128(lds + L_Q + 192 * i + 64 * s + 16 * g);
        f32x4 S1 = z4;
#pragma unroll
        for (int s = 0; s < 3; ++s) S1 = mma32(ones, w4[s], S1);
        const float mean = S1[0] * (1.f / 96.f);
#pragma unroll
        for (int j = 0; j < 12; ++j) { const unsigned w = w4[j >> 2][j & 3]; qn[j >> 2][j & 3] = pk(bflo(w) - mean, bfhi(w) - mean); }
        f32x4 G = z4;
#pragma unroll
        for (int s = 0; s < 3; ++s) G = mma32(qn[s], qn[s], G);
        const float dv = (i & 2) ? ((i & 1) ? G[3] : G[2]) : ((i & 1) ? G[1] : G[0]);
        rstd = LOG2E * __builtin_amdgcn_rsqf(xg_sum(g == (i >> 2) ? dv : 0.f) * (1.f / 96.f) + EPS);
        PIN(rstd);
#pragma unroll
        for (int s = 0; s < 3; ++s) PIN(qn[s]);
      }
      await_ge(fw, 3);
      u32x4 Qb[3];
      {
        u32x4 A[18];
#pragma unroll
        for (int j = 0; j < 18; ++j) { const int s = j / 6, kk = (j % 6) >> 1, h = j & 1; const unsigned tz = s < 2 ? ta0 + 6144 * s : ta2; A[j] = ldstr2(tz + 64 * kk + 8 * h, tz + 768 + 64 * kk + 8 * h); SB(); }
        f32x4 T[6];
#pragma unroll
        for (int j = 0; j < 18; ++j) { T[j % 6] = mma32(A[j], qn[j / 6], j < 6 ? z4 : T[j % 6]); SB(); }
#pragma unroll
        for (int kk = 0; kk < 3; ++kk) {
          const f32x4 x = T[2 * kk] * rstd, y = T[2 * kk + 1] * rstd;
          Qb[kk] = u32x4{pk(x[0], x[1]), pk(x[2], x[3]), pk(y[0], y[1]), pk(y[2], y[3])};
          PIN(Qb[kk]);
        }
      }
      await_ge(fw, 6);
      STAMP(3);
      u32x4 A[18];
#pragma unroll
      for (int j = 0; j < 18; ++j) { const int t = j / 3, kk = j % 3; A[j] = lds128(wa + bandq(3 + (t >> 1)) - bandq(3) + 768 * (t & 1) + 64 * kk); SB(); }
      f32x4 D[6];
#pragma unroll
      for (int j = 0; j < 18; ++j) { D[j / 3] = mma32(A[j], Qb[j % 3], j % 3 == 0 ? z4 : D[j / 3]); SB(); }
      const f32x4 cs = ((D[0] + D[1]) + (D[2] + D[3])) + (D[4] + D[5]);
      const float mu = xg_sum((cs[0] + cs[1]) + (cs[2] + cs[3])) * (1.f / 96.f);
#pragma unroll
      for (int s = 0; s < 3; ++s)
        *(LAS u32x4*)(qa + 1024 * s) =
            u32x4{pk(D[2 * s][0] - mu, D[2 * s][1] - mu), pk(D[2 * s][2] - mu, D[2 * s][3] - mu),
                  pk(D[2 * s + 1][0] - mu, D[2 * s + 1][1] - mu), pk(D[2 * s + 1][2] - mu, D[2 * s + 1][3] - mu)};
      post(f3);
      STAMP(4);
    }
    unsigned epoch = __builtin_amdgcn_raw_buffer_load_b32(rws, 0, WS_EPOCH, 16);
    const int c = b < NCONS ? b : 0;
    qres = *(const GAS u16*)(mk64(ka[4], ka[5]) + (4 * (c / 24) + tp) * 192 + 8 * (c % 24) + 2 * (((tq & 1) << 1) | (tq >> 1)));
    PIN(epoch); PIN(qres);
    *(LAS unsigned*)(lds + L_FL + F_TAG) = max(epoch + 1u, 1u);
    await(lds + L_FL + F_R1 + 4 * tp);
    const __amdgpu_buffer_rsrc_t rwv = rsrc(mk64(ka[10], ka[11]), 18432), rwo = rsrc(mk64(ka[12], ka[13]), 18432);
#pragma unroll
    for (int j = 0; j < 6; ++j) { DMA3("", rwv, 3072 * j, lds + bandv(j >> 1) + 3072 * (j & 1), vl, vl, vl); if (VSLEEP) __builtin_amdgcn_s_sleep(VSLEEP); }
#pragma unroll
    for (int j = 0; j < 6; ++j) { DMA3("", rwo, 3072 * j, lds + bandv(3 + (j >> 1)) + 3072 * (j & 1), vl, vl, vl); if (VSLEEP && j < 5) __builtin_amdgcn_s_sleep(VSLEEP); }
    WAITVM(0);
    post(lds + L_FL + F_V);
    STAMP(5);
  }
  PINS(wave);
  if (wave >= 2) {
    const int s = wave - 2, j = s & 3;
    {
    LANES();
    const int k0 = (s & 6) == 4 ? 0 : (s < 4 ? 1 : 3) + (j >> 1);
    const unsigned fbs = lds + k0 * BLK + 1536 * (j & 1), xbs = lds + k0 * BLK + 3072 + 6144 * (j & 1);
    unsigned aoff = xbs + 192 * i + 16 * (g ^ (((i >> 2) & 1) << 1));
    unsigned foff = fbs + 192 * (i >> 1) + 16 * (g ^ ((i >> 3) << 1));
    const unsigned tb = xbs + 192 * (4 * g + tq) + 8 * tp;
    unsigned tE = tb + 32 * (g & 1), tO = tb - 32 * (g & 1);
    const unsigned fb = fbs + 192 * (2 * g + (tq >> 1)) + 8 * tp;
    unsigned fE = fb + 32 * (g >> 1), fO = fb - 32 * (g >> 1);
    const unsigned ma = lds + L_MR + 4 * i;
    asm volatile(
      "s_mov_b32 s60, 0xaaaaaaaa\n\t"
      "s_mov_b32 s61, 0xaaaaaaaa\n\t"
      "s_mov_b32 s62, 0xcccccccc\n\t"
      "s_mov_b32 s63, 0xcccccccc\n\t"
      "s_mov_b32 s74, 0x00f0000f\n\t"
      "s_mov_b32 s75, 0xf0000f00\n\t"
      "s_mov_b32 s69, 0x42a00000\n\t"
      "s_and_b32 s73, %[s], 3\n\t"
      "s_lshl_b32 s70, s73, 2\n\t"
      "s_add_u32 s70, s70, 161856\n\t"
      "s_lshl_b32 s72, s73, 7\n\t"
      "s_add_u32 s72, s72, 4294803072\n\t"
      "s_lshr_b32 s71, s73, 1\n\t"
      "s_mul_i32 s71, s71, 15360\n\t"
      "s_and_b32 s79, s73, 1\n\t"
      "s_mul_i32 s79, s79, 6144\n\t"
      "s_add_u32 s71, s71, s79\n\t"
      "s_add_u32 s71, s71, 79872\n\t"
      "s_cmp_lt_u32 %[s], 4\n\t"
      "s_cselect_b32 s68, 0, 1\n\t"
      "s_cselect_b32 s76, 8, 18\n\t"
      "s_add_u32 s76, s76, s73\n\t"
      "s_add_u32 s77, s76, 4\n\t"
      "s_mov_b32 s66, 2\n\t"
      "s_mov_b32 s78, 61440\n\t"
      "s_cmp_lt_u32 %[s], 4\n\t"
      "s_cselect_b32 s80, 26, 28\n\t"
      "s_cselect_b32 s81, 30, 34\n\t"
      "s_lshr_b32 s79, s73, 1\n\t"
      "s_add_u32 s80, s80, s79\n\t"
      "s_add_u32 s81, s81, s73\n\t"
      "s_and_b32 s79, %[s], 6\n\t"
      "s_cmp_lg_u32 s79, 4\n\t"
      "s_cbranch_scc1 SET%=\n\t"
      "s_mov_b32 s66, 3\n\t"
      "s_mov_b32 s78, 46080\n\t"
      "s_add_u32 s76, s73, 2\n\t"
      "s_add_u32 s77, s73, 4\n\t"
      "SET%=:\n\t"
      "s_lshl_b32 s82, s73, 2\n\t"
      "s_add_u32 s82, s82, 4294479616\n\t"
      "v_mov_b32 v152, 0xbc2aaaab\n\t"
      "v_cndmask_b32 v158, 0, 1.0, s[74:75]\n\t"
      "v_cndmask_b32 v159, 0, v152, s[74:75]\n\t"
      "v_mov_b32 v36, 0x3f803f80\n\t"
      "v_mov_b32 v37, 0x3f803f80\n\t"
      "v_mov_b32 v38, 0x3f803f80\n\t"
      "v_mov_b32 v39, 0x3f803f80\n\t"
      "v_mov_b64 v[100:101], 0\n\t"
      "v_mov_b64 v[102:103], 0\n\t"
      "v_mov_b64 v[104:105], 0\n\t"
      "v_mov_b64 v[106:107], 0\n\t"
      "v_mov_b64 v[108:109], 0\n\t"
      "v_mov_b64 v[110:111], 0\n\t"
      "v_mov_b64 v[112:113], 0\n\t"
      "v_mov_b64 v[114:115], 0\n\t"
      "v_mov_b64 v[116:117], 0\n\t"
      "v_mov_b64 v[118:119], 0\n\t"
      "v_mov_b64 v[120:121], 0\n\t"
      "v_mov_b64 v[122:123], 0\n\t"
      "v_mov_b32 v150, 0\n\t"
      "v_mov_b32 v151, 1.0\n\t"
      "v_mov_b32 v160, 161856\n\t"
      "s_branch FAb%=\n\t"
      "FAa%=:\n\t"
      "s_sleep 1\n\t"
      "FAb%=:\n\t"
      "ds_read_b32 v152, v160\n\t"
      "s_waitcnt lgkmcnt(0)\n\t"
      "v_cmp_gt_i32 vcc, s76, v152\n\t"
      "s_cbranch_vccnz FAa%=\n\t"
      "ds_read_b128 v[52:55], %[fo] offset:0\n\t"
      "ds_read_b128 v[56:59], %[fo] offset:64\n\t"
      "ds_read_b128 v[60:63], %[fo] offset:128\n\t"
      "ds_read_b128 v[40:43], %[ao] offset:0\n\t"
      "ds_read_b128 v[44:47], %[ao] offset:64\n\t"
      "ds_read_b128 v[48:51], %[ao] offset:128\n\t"
      "v_mov_b32 v32, 0x3a7ba882\n\t"
      "v_mov_b32 v33, 0x3a7ba882\n\t"
      "v_mov_b32 v34, 0x3a7ba882\n\t"
      "v_mov_b32 v35, 0x3a7ba882\n\t"
      "ds_read_b64_tr_b16 v[78:79], %[fE] offset:0\n\t"
      "ds_read_b64_tr_b16 v[82:83], %[fO] offset:32\n\t"
      "ds_read_b64_tr_b16 v[86:87], %[fE] offset:64\n\t"
      "ds_read_b64_tr_b16 v[90:91], %[fO] offset:96\n\t"
      "ds_read_b64_tr_b16 v[94:95], %[fE] offset:128\n\t"
      "ds_read_b64_tr_b16 v[98:99], %[fO] offset:160\n\t"
      "s_waitcnt lgkmcnt(11)\n\t"
      "v_mfma_f32_16x16x32_bf16 v[32:35], v[52:55], v[52:55], v[32:35]\n\t"
      "v_mfma_f32_16x16x32_bf16 v[28:31], v[36:39], v[52:55], 0\n\t"
      "s_waitcnt lgkmcnt(10)\n\t"
      "v_mfma_f32_16x16x32_bf16 v[32:35], v[56:59], v[56:59], v[32:35]\n\t"
      "v_mfma_f32_16x16x32_bf16 v[28:31], v[36:39], v[56:59], v[28:31]\n\t"
      "s_waitcnt lgkmcnt(9)\n\t"
      "v_mfma_f32_16x16x32_bf16 v[32:35], v[60:63], v[60:63], v[32:35]\n\t"
      "v_mfma_f32_16x16x32_bf16 v[28:31], v[36:39], v[60:63], v[28:31]\n\t"
      "s_waitcnt lgkmcnt(8)\n\t"
      "v_mfma_f32_16x16x32_bf16 v[20:23], v[40:43], v[40:43], v[32:35]\n\t"
      "v_mfma_f32_16x16x32_bf16 v[16:19], v[40:43], v[52:55], 0\n\t"
      "s_waitcnt lgkmcnt(7)\n\t"
      "v_mfma_f32_16x16x32_bf16 v[20:23], v[44:47], v[44:47], v[20:23]\n\t"
      "v_mfma_f32_16x16x32_bf16 v[16:19], v[44:47], v[56:59], v[16:19]\n\t"
      "s_waitcnt lgkmcnt(6)\n\t"
      "v_mfma_f32_16x16x32_bf16 v[20:23], v[48:51], v[48:51], v[20:23]\n\t"
      "v_mfma_f32_16x16x32_bf16 v[16:19], v[48:51], v[60:63], v[16:19]\n\t"
      "v_mfma_f32_16x16x32_bf16 v[12:15], v[36:39], v[40:43], v[28:31]\n\t"
      "ds_read_b64_tr_b16 v[76:77], %[tE] offset:0\n\t"
      "ds_read_b64_tr_b16 v[80:81], %[tO] offset:32\n\t"
      "v_mfma_f32_16x16x32_bf16 v[12:15], v[36:39], v[44:47], v[12:15]\n\t"
      "ds_read_b64_tr_b16 v[84:85], %[tE] offset:64\n\t"
      "ds_read_b64_tr_b16 v[88:89], %[tO] offset:96\n\t"
      "v_mfma_f32_16x16x32_bf16 v[12:15], v[36:39], v[48:51], v[12:15]\n\t"
      "ds_read_b64_tr_b16 v[92:93], %[tE] offset:128\n\t"
      "ds_read_b64_tr_b16 v[96:97], %[tO] offset:160\n\t"
      "s_nop 1\n\t"
      "v_fma_f32 v152, 2.0, v16, v20\n\t"
      "v_fma_f32 v153, 2.0, v17, v21\n\t"
      "v_fma_f32 v154, 2.0, v18, v22\n\t"
      "v_fma_f32 v155, 2.0, v19, v23\n\t"
      "v_cndmask_b32 v152, v152, v153, s[60:61]\n\t"
      "v_cndmask_b32 v154, v154, v155, s[60:61]\n\t"
      "v_mul_f32 v153, v159, v12\n\t"
      "v_cndmask_b32 v152, v152, v154, s[62:63]\n\t"
      "v_fmac_f32 v152, v153, v12\n\t"
      "v_max_f32 v152, 0x3a7ba882, v152\n\t"
      "v_rsq_f32 v152, v152\n\t"
      "s_nop 0\n\t"
      "v_mul_f32 v152, v158, v152\n\t"
      "s_nop 1\n\t"
      "v_mfma_f32_16x16x4_f32 v[136:139], v152, v151, 0\n\t"
      "s_branch FBb%=\n\t"
      "FBa%=:\n\t"
      "s_sleep 1\n\t"
      "FBb%=:\n\t"
      "ds_read_b32 v152, v160\n\t"
      "s_waitcnt lgkmcnt(0)\n\t"
      "v_cmp_gt_i32 vcc, s77, v152\n\t"
      "s_cbranch_vccnz FBa%=\n\t"
      "ds_read_b128 v[124:127], %[ao] offset:3072\n\t"
      "ds_read_b128 v[128:131], %[ao] offset:3136\n\t"
      "ds_read_b128 v[132:135], %[ao] offset:3200\n\t"
      "s_waitcnt lgkmcnt(2)\n\t"
      "v_mfma_f32_16x16x32_bf16 v[20:23], v[124:127], v[124:127], v[32:35]\n\t"
      "v_mfma_f32_16x16x32_bf16 v[16:19], v[124:127], v[52:55], 0\n\t"
      "s_waitcnt lgkmcnt(1)\n\t"
      "v_mfma_f32_16x16x32_bf16 v[20:23], v[128:131], v[128:131], v[20:23]\n\t"
      "v_mfma_f32_16x16x32_bf16 v[16:19], v[128:131], v[56:59], v[16:19]\n\t"
      "s_waitcnt lgkmcnt(0)\n\t"
      "v_mfma_f32_16x16x32_bf16 v[20:23], v[132:135], v[132:135], v[20:23]\n\t"
      "v_mfma_f32_16x16x32_bf16 v[16:19], v[132:135], v[60:63], v[16:19]\n\t"
      "v_mfma_f32_16x16x32_bf16 v[12:15], v[36:39], v[124:127], v[28:31]\n\t"
      "v_mfma_f32_16x16x32_bf16 v[12:15], v[36:39], v[128:131], v[12:15]\n\t"
      "v_mfma_f32_16x16x32_bf16 v[12:15], v[36:39], v[132:135], v[12:15]\n\t"
      "s_nop 4\n\t"
      "v_fma_f32 v152, 2.0, v16, v20\n\t"
      "v_fma_f32 v153, 2.0, v17, v21\n\t"
      "v_fma_f32 v154, 2.0, v18, v22\n\t"
      "v_fma_f32 v155, 2.0, v19, v23\n\t"
      "v_cndmask_b32 v152, v152, v153, s[60:61]\n\t"
      "v_cndmask_b32 v154, v154, v155, s[60:61]\n\t"
      "v_mul_f32 v153, v159, v12\n\t"
      "v_cndmask_b32 v152, v152, v154, s[62:63]\n\t"
      "v_fmac_f32 v152, v153, v12\n\t"
      "v_max_f32 v152, 0x3a7ba882, v152\n\t"
      "v_rsq_f32 v152, v152\n\t"
      "s_nop 0\n\t"
      "v_mul_f32 v152, v158, v152\n\t"
      "s_nop 1\n\t"
      "v_mfma_f32_16x16x4_f32 v[140:143], v152, v151, 0\n\t"
      "v_add_u32 v154, 153600, %[vl]\n\t"
      "s_branch QKWb%=\n\t"
      "QKWa%=:\n\t"
      "s_sleep 1\n\t"
      "QKWb%=:\n\t"
      "ds_read_b32 v153, v160 offset:8\n\t"
      "s_waitcnt lgkmcnt(0)\n\t"
      "v_cmp_eq_u32 vcc, 0, v153\n\t"
      "s_cbranch_vccnz QKWa%=\n\t"
      "ds_read_b128 v[64:67], v154 offset:0\n\t"
      "ds_read_b128 v[68:71], v154 offset:1024\n\t"
      "ds_read_b128 v[72:75], v154 offset:2048\n\t"
      "s_waitcnt lgkmcnt(2)\n\t"
      "v_mfma_f32_16x16x32_bf16 v[24:27], v[52:55], v[64:67], 0\n\t"
      "s_waitcnt lgkmcnt(1)\n\t"
      "v_mfma_f32_16x16x32_bf16 v[24:27], v[56:59], v[68:71], v[24:27]\n\t"
      "s_waitcnt lgkmcnt(0)\n\t"
      "v_mfma_f32_16x16x32_bf16 v[24:27], v[60:63], v[72:75], v[24:27]\n\t"
      "v_mfma_f32_16x16x32_bf16 v[8:11], v[40:43], v[64:67], v[24:27]\n\t"
      "v_mfma_f32_16x16x32_bf16 v[8:11], v[44:47], v[68:71], v[8:11]\n\t"
      "v_mfma_f32_16x16x32_bf16 v[8:11], v[48:51], v[72:75], v[8:11]\n\t"
      "v_mfma_f32_16x16x32_bf16 v[144:147], v[124:127], v[64:67], v[24:27]\n\t"
      "v_mfma_f32_16x16x32_bf16 v[144:147], v[128:131], v[68:71], v[144:147]\n\t"
      "v_mfma_f32_16x16x32_bf16 v[144:147], v[132:135], v[72:75], v[144:147]\n\t"
      "s_cmp_lg_u32 %[s], 4\n\t"
      "s_cbranch_scc1 MRW%=\n\t"
      "s_nop 2\n\t"
      "v_mul_f32 v152, v8, v136\n\t"
      "v_mul_f32 v153, v9, v137\n\t"
      "v_mul_f32 v154, v10, v138\n\t"
      "v_mul_f32 v155, v11, v139\n\t"
      "v_max_f32 v152, v152, v153\n\t"
      "v_max3_f32 v152, v154, v155, v152\n\t"
      "v_mov_b32 v153, v152\n\t"
      "s_nop 1\n\t"
      "v_permlane16_swap_b32 v152, v153\n\t"
      "v_max_f32 v152, v152, v153\n\t"
      "v_mov_b32 v153, v152\n\t"
      "s_nop 1\n\t"
      "v_permlane32_swap_b32 v152, v153\n\t"
      "v_max_f32 v152, v152, v153\n\t"
      "v_min_u32 v152, 0xfffffffe, v152\n\t"
      "ds_write_b32 %[ma], v152\n\t"
      "MRW%=:\n\t"
      "MREa%=:\n\t"
      "ds_read_b32 v148, %[ma]\n\t"
      "s_waitcnt lgkmcnt(0)\n\t"
      "v_cmp_eq_u32 vcc, -1, v148\n\t"
      "s_cbranch_vccnz MREa%=\n\t"
      "v_fma_f32 v8, v8, v136, -v148\n\t"
      "v_fma_f32 v9, v9, v137, -v148\n\t"
      "v_fma_f32 v10, v10, v138, -v148\n\t"
      "v_fma_f32 v11, v11, v139, -v148\n\t"
      "v_max_f32 v156, v8, v9\n\t"
      "v_max3_f32 v156, v10, v11, v156\n\t"
      "v_cmp_lt_f32 vcc, s69, v156\n\t"
      "v_exp_f32 v12, v8\n\t"
      "v_exp_f32 v13, v9\n\t"
      "v_exp_f32 v14, v10\n\t"
      "v_exp_f32 v15, v11\n\t"
      "s_cbranch_vccnz SLC%=\n\t"
      "OKC%=:\n\t"
      "v_mul_f32 v136, v12, v136\n\t"
      "v_mul_f32 v137, v13, v137\n\t"
      "v_mul_f32 v138, v14, v138\n\t"
      "v_mul_f32 v139, v15, v139\n\t"
      "v_cvt_pk_bf16_f32 v20, v136, v137\n\t"
      "v_cvt_pk_bf16_f32 v21, v138, v139\n\t"
      "v_cvt_pk_bf16_f32 v22, v136, v137\n\t"
      "v_cvt_pk_bf16_f32 v23, v138, v139\n\t"
      "v_add_f32 v150, v150, v15\n\t"
      "s_waitcnt lgkmcnt(0)\n\t"
      "v_mfma_f32_16x16x32_bf16 v[100:103], v[76:79], v[20:23], v[100:103]\n\t"
      "v_add_f32 v150, v150, v12\n\t"
      "v_mfma_f32_16x16x32_bf16 v[104:107], v[80:83], v[20:23], v[104:107]\n\t"
      "v_add_f32 v150, v150, v13\n\t"
      "v_mfma_f32_16x16x32_bf16 v[108:111], v[84:87], v[20:23], v[108:111]\n\t"
      "v_add_f32 v150, v150, v14\n\t"
      "v_mfma_f32_16x16x32_bf16 v[112:115], v[88:91], v[20:23], v[112:115]\n\t"
      "v_mfma_f32_16x16x32_bf16 v[116:119], v[92:95], v[20:23], v[116:119]\n\t"
      "v_mfma_f32_16x16x32_bf16 v[120:123], v[96:99], v[20:23], v[120:123]\n\t"
      "ds_read_b64_tr_b16 v[76:77], %[tE] offset:3072\n\t"
      "ds_read_b64_tr_b16 v[80:81], %[tO] offset:3104\n\t"
      "ds_read_b64_tr_b16 v[84:85], %[tE] offset:3136\n\t"
      "ds_read_b64_tr_b16 v[88:89], %[tO] offset:3168\n\t"
      "ds_read_b64_tr_b16 v[92:93], %[tE] offset:3200\n\t"
      "ds_read_b64_tr_b16 v[96:97], %[tO] offset:3232\n\t"
      "v_fma_f32 v144, v144, v140, -v148\n\t"
      "v_fma_f32 v145, v145, v141, -v148\n\t"
      "v_fma_f32 v146, v146, v142, -v148\n\t"
      "v_fma_f32 v147, v147, v143, -v148\n\t"
      "v_max_f32 v156, v144, v145\n\t"
      "v_max3_f32 v156, v146, v147, v156\n\t"
      "v_cmp_lt_f32 vcc, s69, v156\n\t"
      "v_exp_f32 v12, v144\n\t"
      "v_exp_f32 v13, v145\n\t"
      "v_exp_f32 v14, v146\n\t"
      "v_exp_f32 v15, v147\n\t"
      "s_cbranch_vccnz SLD%=\n\t"
      "OKD%=:\n\t"
      "v_mul_f32 v140, v12, v140\n\t"
      "v_mul_f32 v141, v13, v141\n\t"
      "v_mul_f32 v142, v14, v142\n\t"
      "v_mul_f32 v143, v15, v143\n\t"
      "v_cvt_pk_bf16_f32 v20, v140, v141\n\t"
      "v_cvt_pk_bf16_f32 v21, v142, v143\n\t"
      "v_cvt_pk_bf16_f32 v22, v140, v141\n\t"
      "v_cvt_pk_bf16_f32 v23, v142, v143\n\t"
      "v_add_f32 v150, v150, v15\n\t"
      "s_waitcnt lgkmcnt(0)\n\t"
      "v_mfma_f32_16x16x32_bf16 v[100:103], v[76:79], v[20:23], v[100:103]\n\t"
      "v_add_f32 v150, v150, v12\n\t"
      "v_mfma_f32_16x16x32_bf16 v[104:107], v[80:83], v[20:23], v[104:107]\n\t"
      "v_add_f32 v150, v150, v13\n\t"
      "v_mfma_f32_16x16x32_bf16 v[108:111], v[84:87], v[20:23], v[108:111]\n\t"
      "v_add_f32 v150, v150, v14\n\t"
      "v_mfma_f32_16x16x32_bf16 v[112:115], v[88:91], v[20:23], v[112:115]\n\t"
      "v_mfma_f32_16x16x32_bf16 v[116:119], v[92:95], v[20:23], v[116:119]\n\t"
      "v_mfma_f32_16x16x32_bf16 v[120:123], v[96:99], v[20:23], v[120:123]\n\t"
      "v_add_u32 %[ao], s78, %[ao]\n\t"
      "v_add_u32 %[tE], s78, %[tE]\n\t"
      "v_add_u32 %[tO], s78, %[tO]\n\t"
      "v_add_u32 %[fo], s78, %[fo]\n\t"
      "v_add_u32 %[fE], s78, %[fE]\n\t"
      "v_add_u32 %[fO], s78, %[fO]\n\t"
      "s_mov_b32 s67, s80\n\t"
      "s_mov_b32 s76, s81\n\t"
      "s_add_u32 s77, s81, 8\n\t"
      "s_sub_u32 s66, s66, 1\n\t"
      "s_cmp_lg_u32 s68, 0\n\t"
      "s_cbranch_scc1 NX2%=\n\t"
      "v_mov_b32 v154, s70\n\t"
      "v_mov_b32 v155, 1\n\t"
      "ds_write_b32 v154, v155 offset:48\n\t"
      "NX2%=:\n\t"
      "s_cmp_eq_u32 s66, 2\n\t"
      "s_cbranch_scc0 LOOP%=\n\t"
      "s_mov_b32 s67, 16\n\t"
      "s_sub_u32 s76, s81, 16\n\t"
      "s_sub_u32 s77, s81, 12\n\t"
      "s_mov_b32 s78, 61440\n\t"
      "s_branch LOOP%=\n\t"
      "SLA%=:\n\t"
      "v_mov_b32 v152, v156\n\t"
      "s_nop 1\n\t"
      "v_permlane16_swap_b32 v156, v152\n\t"
      "v_max_f32 v156, v156, v152\n\t"
      "v_mov_b32 v152, v156\n\t"
      "s_nop 1\n\t"
      "v_permlane32_swap_b32 v156, v152\n\t"
      "v_max_f32 v156, v156, v152\n\t"
      "v_max_f32 v156, 0, v156\n\t"
      "v_exp_f32 v152, -v156\n\t"
      "v_add_f32 v148, v148, v156\n\t"
      "v_mul_f32 v150, v150, v152\n\t"
      "v_pk_mul_f32 v[100:101], v[100:101], v[152:153] op_sel_hi:[1,0]\n\t"
      "v_pk_mul_f32 v[102:103], v[102:103], v[152:153] op_sel_hi:[1,0]\n\t"
      "v_pk_mul_f32 v[104:105], v[104:105], v[152:153] op_sel_hi:[1,0]\n\t"
      "v_pk_mul_f32 v[106:107], v[106:107], v[152:153] op_sel_hi:[1,0]\n\t"
      "v_pk_mul_f32 v[108:109], v[108:109], v[152:153] op_sel_hi:[1,0]\n\t"
      "v_pk_mul_f32 v[110:111], v[110:111], v[152:153] op_sel_hi:[1,0]\n\t"
      "v_pk_mul_f32 v[112:113], v[112:113], v[152:153] op_sel_hi:[1,0]\n\t"
      "v_pk_mul_f32 v[114:115], v[114:115], v[152:153] op_sel_hi:[1,0]\n\t"
      "v_pk_mul_f32 v[116:117], v[116:117], v[152:153] op_sel_hi:[1,0]\n\t"
      "v_pk_mul_f32 v[118:119], v[118:119], v[152:153] op_sel_hi:[1,0]\n\t"
      "v_pk_mul_f32 v[120:121], v[120:121], v[152:153] op_sel_hi:[1,0]\n\t"
      "v_pk_mul_f32 v[122:123], v[122:123], v[152:153] op_sel_hi:[1,0]\n\t"
      "v_sub_f32 v8, v8, v156\n\t"
      "v_sub_f32 v9, v9, v156\n\t"
      "v_sub_f32 v10, v10, v156\n\t"
      "v_sub_f32 v11, v11, v156\n\t"
      "v_exp_f32 v12, v8\n\t"
      "v_exp_f32 v13, v9\n\t"
      "v_exp_f32 v14, v10\n\t"
      "v_exp_f32 v15, v11\n\t"
      "v_mov_b32 v153, 1\n\t"
      "ds_write_b32 v160, v153 offset:16\n\t"
      "s_branch OKA%=\n\t"
      "SLB%=:\n\t"
      "v_mov_b32 v152, v156\n\t"
      "s_nop 1\n\t"
      "v_permlane16_swap_b32 v156, v152\n\t"
      "v_max_f32 v156, v156, v152\n\t"
      "v_mov_b32 v152, v156\n\t"
      "s_nop 1\n\t"
      "v_permlane32_swap_b32 v156, v152\n\t"
      "v_max_f32 v156, v156, v152\n\t"
      "v_max_f32 v156, 0, v156\n\t"
      "v_exp_f32 v152, -v156\n\t"
      "v_add_f32 v148, v148, v156\n\t"
      "v_mul_f32 v150, v150, v152\n\t"
      "v_pk_mul_f32 v[100:101], v[100:101], v[152:153] op_sel_hi:[1,0]\n\t"
      "v_pk_mul_f32 v[102:103], v[102:103], v[152:153] op_sel_hi:[1,0]\n\t"
      "v_pk_mul_f32 v[104:105], v[104:105], v[152:153] op_sel_hi:[1,0]\n\t"
      "v_pk_mul_f32 v[106:107], v[106:107], v[152:153] op_sel_hi:[1,0]\n\t"
      "v_pk_mul_f32 v[108:109], v[108:109], v[152:153] op_sel_hi:[1,0]\n\t"
      "v_pk_mul_f32 v[110:111], v[110:111], v[152:153] op_sel_hi:[1,0]\n\t"
      "v_pk_mul_f32 v[112:113], v[112:113], v[152:153] op_sel_hi:[1,0]\n\t"
      "v_pk_mul_f32 v[114:115], v[114:115], v[152:153] op_sel_hi:[1,0]\n\t"
      "v_pk_mul_f32 v[116:117], v[116:117], v[152:153] op_sel_hi:[1,0]\n\t"
      "v_pk_mul_f32 v[118:119], v[118:119], v[152:153] op_sel_hi:[1,0]\n\t"
      "v_pk_mul_f32 v[120:121], v[120:121], v[152:153] op_sel_hi:[1,0]\n\t"
      "v_pk_mul_f32 v[122:123], v[122:123], v[152:153] op_sel_hi:[1,0]\n\t"
      "v_sub_f32 v8, v8, v156\n\t"
      "v_sub_f32 v9, v9, v156\n\t"
      "v_sub_f32 v10, v10, v156\n\t"
      "v_sub_f32 v11, v11, v156\n\t"
      "v_exp_f32 v12, v8\n\t"
      "v_exp_f32 v13, v9\n\t"
      "v_exp_f32 v14, v10\n\t"
      "v_exp_f32 v15, v11\n\t"
      "v_mov_b32 v153, 1\n\t"
      "ds_write_b32 v160, v153 offset:16\n\t"
      "s_branch OKB%=\n\t"
      "SLC%=:\n\t"
      "v_mov_b32 v152, v156\n\t"
      "s_nop 1\n\t"
      "v_permlane16_swap_b32 v156, v152\n\t"
      "v_max_f32 v156, v156, v152\n\t"
      "v_mov_b32 v152, v156\n\t"
      "s_nop 1\n\t"
      "v_permlane32_swap_b32 v156, v152\n\t"
      "v_max_f32 v156, v156, v152\n\t"
      "v_max_f32 v156, 0, v156\n\t"
      "v_exp_f32 v152, -v156\n\t"
      "v_add_f32 v148, v148, v156\n\t"
      "v_mul_f32 v150, v150, v152\n\t"
      "v_pk_mul_f32 v[100:101], v[100:101], v[152:153] op_sel_hi:[1,0]\n\t"
      "v_pk_mul_f32 v[102:103], v[102:103], v[152:153] op_sel_hi:[1,0]\n\t"
      "v_pk_mul_f32 v[104:105], v[104:105], v[152:153] op_sel_hi:[1,0]\n\t"
      "v_pk_mul_f32 v[106:107], v[106:107], v[152:153] op_sel_hi:[1,0]\n\t"
      "v_pk_mul_f32 v[108:109], v[108:109], v[152:153] op_sel_hi:[1,0]\n\t"
      "v_pk_mul_f32 v[110:111], v[110:111], v[152:153] op_sel_hi:[1,0]\n\t"
      "v_pk_mul_f32 v[112:113], v[112:113], v[152:153] op_sel_hi:[1,0]\n\t"
      "v_pk_mul_f32 v[114:115], v[114:115], v[152:153] op_sel_hi:[1,0]\n\t"
      "v_pk_mul_f32 v[116:117], v[116:117], v[152:153] op_sel_hi:[1,0]\n\t"
      "v_pk_mul_f32 v[118:119], v[118:119], v[152:153] op_sel_hi:[1,0]\n\t"
      "v_pk_mul_f32 v[120:121], v[120:121], v[152:153] op_sel_hi:[1,0]\n\t"
      "v_pk_mul_f32 v[122:123], v[122:123], v[152:153] op_sel_hi:[1,0]\n\t"
      "v_sub_f32 v8, v8, v156\n\t"
      "v_sub_f32 v9, v9, v156\n\t"
      "v_sub_f32 v10, v10, v156\n\t"
      "v_sub_f32 v11, v11, v156\n\t"
      "v_exp_f32 v12, v8\n\t"
      "v_exp_f32 v13, v9\n\t"
      "v_exp_f32 v14, v10\n\t"
      "v_exp_f32 v15, v11\n\t"
      "v_mov_b32 v153, 1\n\t"
      "ds_write_b32 v160, v153 offset:16\n\t"
      "s_branch OKC%=\n\t"
      "SLD%=:\n\t"
      "v_mov_b32 v152, v156\n\t"
      "s_nop 1\n\t"
      "v_permlane16_swap_b32 v156, v152\n\t"
      "v_max_f32 v156, v156, v152\n\t"
      "v_mov_b32 v152, v156\n\t"
      "s_nop 1\n\t"
      "v_permlane32_swap_b32 v156, v152\n\t"
      "v_max_f32 v156, v156, v152\n\t"
      "v_max_f32 v156, 0, v156\n\t"
      "v_exp_f32 v152, -v156\n\t"
      "v_add_f32 v148, v148, v156\n\t"
      "v_mul_f32 v150, v150, v152\n\t"
      "v_pk_mul_f32 v[100:101], v[100:101], v[152:153] op_sel_hi:[1,0]\n\t"
      "v_pk_mul_f32 v[102:103], v[102:103], v[152:153] op_sel_hi:[1,0]\n\t"
      "v_pk_mul_f32 v[104:105], v[104:105], v[152:153] op_sel_hi:[1,0]\n\t"
      "v_pk_mul_f32 v[106:107], v[106:107], v[152:153] op_sel_hi:[1,0]\n\t"
      "v_pk_mul_f32 v[108:109], v[108:109], v[152:153] op_sel_hi:[1,0]\n\t"
      "v_pk_mul_f32 v[110:111], v[110:111], v[152:153] op_sel_hi:[1,0]\n\t"
      "v_pk_mul_f32 v[112:113], v[112:113], v[152:153] op_sel_hi:[1,0]\n\t"
      "v_pk_mul_f32 v[114:115], v[114:115], v[152:153] op_sel_hi:[1,0]\n\t"
      "v_pk_mul_f32 v[116:117], v[116:117], v[152:153] op_sel_hi:[1,0]\n\t"
      "v_pk_mul_f32 v[118:119], v[118:119], v[152:153] op_sel_hi:[1,0]\n\t"
      "v_pk_mul_f32 v[120:121], v[120:121], v[152:153] op_sel_hi:[1,0]\n\t"
      "v_pk_mul_f32 v[122:123], v[122:123], v[152:153] op_sel_hi:[1,0]\n\t"
      "v_sub_f32 v144, v144, v156\n\t"
      "v_sub_f32 v145, v145, v156\n\t"
      "v_sub_f32 v146, v146, v156\n\t"
      "v_sub_f32 v147, v147, v156\n\t"
      "v_exp_f32 v12, v144\n\t"
      "v_exp_f32 v13, v145\n\t"
      "v_exp_f32 v14, v146\n\t"
      "v_exp_f32 v15, v147\n\t"
      "v_mov_b32 v153, 1\n\t"
      "ds_write_b32 v160, v153 offset:16\n\t"
      "s_branch OKD%=\n\t"
      "LOOP%=:\n\t"
      "s_branch WFb%=\n\t"
      "WFa%=:\n\t"
      "s_sleep 1\n\t"
      "WFb%=:\n\t"
      "ds_read_b32 v152, v160\n\t"
      "s_waitcnt lgkmcnt(0)\n\t"
      "v_cmp_gt_i32 vcc, s67, v152\n\t"
      "s_cbranch_vccnz WFa%=\n\t"
      "ds_read_b128 v[52:55], %[fo] offset:0\n\t"
      "ds_read_b128 v[56:59], %[fo] offset:64\n\t"
      "ds_read_b128 v[60:63], %[fo] offset:128\n\t"
      "v_mov_b32 v32, 0x3a7ba882\n\t"
      "v_mov_b32 v33, 0x3a7ba882\n\t"
      "v_mov_b32 v34, 0x3a7ba882\n\t"
      "v_mov_b32 v35, 0x3a7ba882\n\t"
      "ds_read_b64_tr_b16 v[78:79], %[fE] offset:0\n\t"
      "ds_read_b64_tr_b16 v[82:83], %[fO] offset:32\n\t"
      "ds_read_b64_tr_b16 v[86:87], %[fE] offset:64\n\t"
      "ds_read_b64_tr_b16 v[90:91], %[fO] offset:96\n\t"
      "ds_read_b64_tr_b16 v[94:95], %[fE] offset:128\n\t"
      "ds_read_b64_tr_b16 v[98:99], %[fO] offset:160\n\t"
      "s_waitcnt lgkmcnt(8)\n\t"
      "v_mfma_f32_16x16x32_bf16 v[32:35], v[52:55], v[52:55], v[32:35]\n\t"
      "v_mfma_f32_16x16x32_bf16 v[28:31], v[36:39], v[52:55], 0\n\t"
      "v_mfma_f32_16x16x32_bf16 v[24:27], v[52:55], v[64:67], 0\n\t"
      "s_waitcnt lgkmcnt(7)\n\t"
      "v_mfma_f32_16x16x32_bf16 v[32:35], v[56:59], v[56:59], v[32:35]\n\t"
      "v_mfma_f32_16x16x32_bf16 v[28:31], v[36:39], v[56:59], v[28:31]\n\t"
      "v_mfma_f32_16x16x32_bf16 v[24:27], v[56:59], v[68:71], v[24:27]\n\t"
      "s_waitcnt lgkmcnt(6)\n\t"
      "v_mfma_f32_16x16x32_bf16 v[32:35], v[60:63], v[60:63], v[32:35]\n\t"
      "v_mfma_f32_16x16x32_bf16 v[28:31], v[36:39], v[60:63], v[28:31]\n\t"
      "v_mfma_f32_16x16x32_bf16 v[24:27], v[60:63], v[72:75], v[24:27]\n\t"
      "WAa%=:\n\t"
      "ds_read_b32 v152, v160\n\t"
      "s_waitcnt lgkmcnt(0)\n\t"
      "v_cmp_gt_i32 vcc, s76, v152\n\t"
      "s_cbranch_vccnz WAa%=\n\t"
      "ds_read_b128 v[40:43], %[ao] offset:0\n\t"
      "ds_read_b128 v[44:47], %[ao] offset:64\n\t"
      "ds_read_b128 v[48:51], %[ao] offset:128\n\t"
      "ds_read_b64_tr_b16 v[76:77], %[tE] offset:0\n\t"
      "ds_read_b64_tr_b16 v[80:81], %[tO] offset:32\n\t"
      "ds_read_b64_tr_b16 v[84:85], %[tE] offset:64\n\t"
      "ds_read_b64_tr_b16 v[88:89], %[tO] offset:96\n\t"
      "ds_read_b64_tr_b16 v[92:93], %[tE] offset:128\n\t"
      "ds_read_b64_tr_b16 v[96:97], %[tO] offset:160\n\t"
      "s_waitcnt lgkmcnt(8)\n\t"
      "v_mfma_f32_16x16x32_bf16 v[20:23], v[40:43], v[40:43], v[32:35]\n\t"
      "v_mfma_f32_16x16x32_bf16 v[16:19], v[40:43], v[52:55], 0\n\t"
      "s_waitcnt lgkmcnt(7)\n\t"
      "v_mfma_f32_16x16x32_bf16 v[20:23], v[44:47], v[44:47], v[20:23]\n\t"
      "v_mfma_f32_16x16x32_bf16 v[16:19], v[44:47], v[56:59], v[16:19]\n\t"
      "s_waitcnt lgkmcnt(6)\n\t"
      "v_mfma_f32_16x16x32_bf16 v[20:23], v[48:51], v[48:51], v[20:23]\n\t"
      "v_mfma_f32_16x16x32_bf16 v[16:19], v[48:51], v[60:63], v[16:19]\n\t"
      "v_mfma_f32_16x16x32_bf16 v[12:15], v[36:39], v[40:43], v[28:31]\n\t"
      "v_mfma_f32_16x16x32_bf16 v[12:15], v[36:39], v[44:47], v[12:15]\n\t"
      "v_mfma_f32_16x16x32_bf16 v[12:15], v[36:39], v[48:51], v[12:15]\n\t"
      "s_nop 4\n\t"
      "v_fma_f32 v152, 2.0, v16, v20\n\t"
      "v_fma_f32 v153, 2.0, v17, v21\n\t"
      "v_fma_f32 v154, 2.0, v18, v22\n\t"
      "v_fma_f32 v155, 2.0, v19, v23\n\t"
      "v_mfma_f32_16x16x32_bf16 v[8:11], v[40:43], v[64:67], v[24:27]\n\t"
      "v_cndmask_b32 v152, v152, v153, s[60:61]\n\t"
      "v_cndmask_b32 v154, v154, v155, s[60:61]\n\t"
      "v_mfma_f32_16x16x32_bf16 v[8:11], v[44:47], v[68:71], v[8:11]\n\t"
      "v_mul_f32 v153, v159, v12\n\t"
      "v_cndmask_b32 v152, v152, v154, s[62:63]\n\t"
      "v_mfma_f32_16x16x32_bf16 v[8:11], v[48:51], v[72:75], v[8:11]\n\t"
      "v_fmac_f32 v152, v153, v12\n\t"
      "v_max_f32 v152, 0x3a7ba882, v152\n\t"
      "v_rsq_f32 v152, v152\n\t"
      "s_nop 0\n\t"
      "v_mul_f32 v152, v158, v152\n\t"
      "s_nop 1\n\t"
      "v_mfma_f32_16x16x4_f32 v[16:19], v152, v151, 0\n\t"
      "v_add_u32 %[fo], s78, %[fo]\n\t"
      "v_add_u32 %[fE], s78, %[fE]\n\t"
      "v_add_u32 %[fO], s78, %[fO]\n\t"
      "s_nop 6\n\t"
      "v_fma_f32 v8, v8, v16, -v148\n\t"
      "v_fma_f32 v9, v9, v17, -v148\n\t"
      "v_fma_f32 v10, v10, v18, -v148\n\t"
      "v_fma_f32 v11, v11, v19, -v148\n\t"
      "v_max_f32 v156, v8, v9\n\t"
      "v_max3_f32 v156, v10, v11, v156\n\t"
      "v_cmp_lt_f32 vcc, s69, v156\n\t"
      "v_exp_f32 v12, v8\n\t"
      "v_exp_f32 v13, v9\n\t"
      "v_exp_f32 v14, v10\n\t"
      "v_exp_f32 v15, v11\n\t"
      "s_cbranch_vccnz SLA%=\n\t"
      "OKA%=:\n\t"
      "v_mul_f32 v16, v12, v16\n\t"
      "v_mul_f32 v17, v13, v17\n\t"
      "v_mul_f32 v18, v14, v18\n\t"
      "v_mul_f32 v19, v15, v19\n\t"
      "v_cvt_pk_bf16_f32 v20, v16, v17\n\t"
      "v_cvt_pk_bf16_f32 v21, v18, v19\n\t"
      "v_cvt_pk_bf16_f32 v22, v16, v17\n\t"
      "v_cvt_pk_bf16_f32 v23, v18, v19\n\t"
      "v_add_f32 v150, v150, v15\n\t"
      "s_waitcnt lgkmcnt(0)\n\t"
      "v_mfma_f32_16x16x32_bf16 v[100:103], v[76:79], v[20:23], v[100:103]\n\t"
      "v_add_f32 v150, v150, v12\n\t"
      "v_mfma_f32_16x16x32_bf16 v[104:107], v[80:83], v[20:23], v[104:107]\n\t"
      "v_add_f32 v150, v150, v13\n\t"
      "v_mfma_f32_16x16x32_bf16 v[108:111], v[84:87], v[20:23], v[108:111]\n\t"
      "v_add_f32 v150, v150, v14\n\t"
      "v_mfma_f32_16x16x32_bf16 v[112:115], v[88:91], v[20:23], v[112:115]\n\t"
      "v_mfma_f32_16x16x32_bf16 v[116:119], v[92:95], v[20:23], v[116:119]\n\t"
      "v_mfma_f32_16x16x32_bf16 v[120:123], v[96:99], v[20:23], v[120:123]\n\t"
      "WBa%=:\n\t"
      "ds_read_b32 v152, v160\n\t"
      "s_waitcnt lgkmcnt(0)\n\t"
      "v_cmp_gt_i32 vcc, s77, v152\n\t"
      "s_cbranch_vccnz WBa%=\n\t"
      "ds_read_b128 v[40:43], %[ao] offset:3072\n\t"
      "ds_read_b128 v[44:47], %[ao] offset:3136\n\t"
      "ds_read_b128 v[48:51], %[ao] offset:3200\n\t"
      "ds_read_b64_tr_b16 v[76:77], %[tE] offset:3072\n\t"
      "ds_read_b64_tr_b16 v[80:81], %[tO] offset:3104\n\t"
      "ds_read_b64_tr_b16 v[84:85], %[tE] offset:3136\n\t"
      "ds_read_b64_tr_b16 v[88:89], %[tO] offset:3168\n\t"
      "ds_read_b64_tr_b16 v[92:93], %[tE] offset:3200\n\t"
      "ds_read_b64_tr_b16 v[96:97], %[tO] offset:3232\n\t"
      "s_mov_b32 s67, s80\n\t"
      "s_mov_b32 s76, s81\n\t"
      "s_add_u32 s77, s81, 8\n\t"
      "s_sub_u32 s66, s66, 1\n\t"
      "s_waitcnt lgkmcnt(8)\n\t"
      "v_mfma_f32_16x16x32_bf16 v[20:23], v[40:43], v[40:43], v[32:35]\n\t"
      "v_mfma_f32_16x16x32_bf16 v[16:19], v[40:43], v[52:55], 0\n\t"
      "s_waitcnt lgkmcnt(7)\n\t"
      "v_mfma_f32_16x16x32_bf16 v[20:23], v[44:47], v[44:47], v[20:23]\n\t"
      "v_mfma_f32_16x16x32_bf16 v[16:19], v[44:47], v[56:59], v[16:19]\n\t"
      "s_waitcnt lgkmcnt(6)\n\t"
      "v_mfma_f32_16x16x32_bf16 v[20:23], v[48:51], v[48:51], v[20:23]\n\t"
      "v_mfma_f32_16x16x32_bf16 v[16:19], v[48:51], v[60:63], v[16:19]\n\t"
      "v_mfma_f32_16x16x32_bf16 v[12:15], v[36:39], v[40:43], v[28:31]\n\t"
      "v_mfma_f32_16x16x32_bf16 v[12:15], v[36:39], v[44:47], v[12:15]\n\t"
      "v_mfma_f32_16x16x32_bf16 v[12:15], v[36:39], v[48:51], v[12:15]\n\t"
      "s_nop 4\n\t"
      "v_fma_f32 v152, 2.0, v16, v20\n\t"
      "v_fma_f32 v153, 2.0, v17, v21\n\t"
      "v_fma_f32 v154, 2.0, v18, v22\n\t"
      "v_fma_f32 v155, 2.0, v19, v23\n\t"
      "v_mfma_f32_16x16x32_bf16 v[8:11], v[40:43], v[64:67], v[24:27]\n\t"
      "v_cndmask_b32 v152, v152, v153, s[60:61]\n\t"
      "v_cndmask_b32 v154, v154, v155, s[60:61]\n\t"
      "v_mfma_f32_16x16x32_bf16 v[8:11], v[44:47], v[68:71], v[8:11]\n\t"
      "v_mul_f32 v153, v159, v12\n\t"
      "v_cndmask_b32 v152, v152, v154, s[62:63]\n\t"
      "v_mfma_f32_16x16x32_bf16 v[8:11], v[48:51], v[72:75], v[8:11]\n\t"
      "v_fmac_f32 v152, v153, v12\n\t"
      "v_max_f32 v152, 0x3a7ba882, v152\n\t"
      "v_rsq_f32 v152, v152\n\t"
      "s_nop 0\n\t"
      "v_mul_f32 v152, v158, v152\n\t"
      "s_nop 1\n\t"
      "v_mfma_f32_16x16x4_f32 v[16:19], v152, v151, 0\n\t"
      "v_add_u32 %[ao], s78, %[ao]\n\t"
      "v_add_u32 %[tE], s78, %[tE]\n\t"
      "v_add_u32 %[tO], s78, %[tO]\n\t"
      "v_add_u32 v24, s71, %[vl]\n\t"
      "v_lshlrev_b32 v25, 1, %[ma]\n\t"
      "v_mov_b32 v26, s70\n\t"
      "v_add_u32 v25, s72, v25\n\t"
      "v_lshl_add_u32 v28, %[ma], 2, s82\n\t"
      "v_add_u32 v27, 30720, v24\n\t"
      "s_cmp_lg_u32 s66, 0\n\t"
      "v_fma_f32 v8, v8, v16, -v148\n\t"
      "v_fma_f32 v9, v9, v17, -v148\n\t"
      "v_fma_f32 v10, v10, v18, -v148\n\t"
      "v_fma_f32 v11, v11, v19, -v148\n\t"
      "v_max_f32 v156, v8, v9\n\t"
      "v_max3_f32 v156, v10, v11, v156\n\t"
      "v_cmp_lt_f32 vcc, s69, v156\n\t"
      "v_exp_f32 v12, v8\n\t"
      "v_exp_f32 v13, v9\n\t"
      "v_exp_f32 v14, v10\n\t"
      "v_exp_f32 v15, v11\n\t"
      "s_cbranch_vccnz SLB%=\n\t"
      "OKB%=:\n\t"
      "v_mul_f32 v16, v12, v16\n\t"
      "v_mul_f32 v17, v13, v17\n\t"
      "v_mul_f32 v18, v14, v18\n\t"
      "v_mul_f32 v19, v15, v19\n\t"
      "v_cvt_pk_bf16_f32 v20, v16, v17\n\t"
      "v_cvt_pk_bf16_f32 v21, v18, v19\n\t"
      "v_cvt_pk_bf16_f32 v22, v16, v17\n\t"
      "v_cvt_pk_bf16_f32 v23, v18, v19\n\t"
      "v_add_f32 v150, v150, v15\n\t"
      "s_waitcnt lgkmcnt(0)\n\t"
      "v_mfma_f32_16x16x32_bf16 v[100:103], v[76:79], v[20:23], v[100:103]\n\t"
      "v_add_f32 v150, v150, v12\n\t"
      "v_mfma_f32_16x16x32_bf16 v[104:107], v[80:83], v[20:23], v[104:107]\n\t"
      "v_add_f32 v150, v150, v13\n\t"
      "v_mfma_f32_16x16x32_bf16 v[108:111], v[84:87], v[20:23], v[108:111]\n\t"
      "v_add_f32 v150, v150, v14\n\t"
      "v_mfma_f32_16x16x32_bf16 v[112:115], v[88:91], v[20:23], v[112:115]\n\t"
      "v_mfma_f32_16x16x32_bf16 v[116:119], v[92:95], v[20:23], v[116:119]\n\t"
      "v_mfma_f32_16x16x32_bf16 v[120:123], v[96:99], v[20:23], v[120:123]\n\t"
      "s_cbranch_scc1 LOOP%=\n\t"
      "s_cmp_eq_u32 s68, 0\n\t"
      "s_cbranch_scc1 AEND%=\n\t"
      "ds_read_b32 v153, v26 offset:32\n\t"
      "ds_read_b64 v[154:155], v25\n\t"
      "ds_read_b128 v[124:127], v24 offset:0\n\t"
      "ds_read_b128 v[128:131], v24 offset:1024\n\t"
      "ds_read_b128 v[132:135], v24 offset:2048\n\t"
      "ds_read_b128 v[136:139], v24 offset:3072\n\t"
      "ds_read_b128 v[140:143], v24 offset:4096\n\t"
      "ds_read_b128 v[144:147], v24 offset:5120\n\t"
      "v_mov_b32 v152, v150\n\t"
      "s_nop 1\n\t"
      "v_permlane16_swap_b32 v150, v152\n\t"
      "v_add_f32 v150, v150, v152\n\t"
      "v_mov_b32 v152, v150\n\t"
      "s_nop 1\n\t"
      "v_permlane32_swap_b32 v150, v152\n\t"
      "v_add_f32 v150, v150, v152\n\t"
      "s_waitcnt lgkmcnt(6)\n\t"
      "v_cmp_eq_u32 vcc, 0, v153\n\t"
      "v_cmp_neq_f32 s[64:65], v148, v154\n\t"
      "s_or_b64 vcc, vcc, s[64:65]\n\t"
      "s_cbranch_vccnz ABX%=\n\t"
      "ABK%=:\n\t"
      "v_add_f32 v150, v150, v155\n\t"
      "s_waitcnt lgkmcnt(4)\n\t"
      "v_add_f32 v100, v100, v124\n\t"
      "v_add_f32 v101, v101, v125\n\t"
      "v_add_f32 v102, v102, v126\n\t"
      "v_add_f32 v103, v103, v127\n\t"
      "v_add_f32 v104, v104, v128\n\t"
      "v_add_f32 v105, v105, v129\n\t"
      "v_add_f32 v106, v106, v130\n\t"
      "v_add_f32 v107, v107, v131\n\t"
      "v_cvt_pk_bf16_f32 v40, v100, v101\n\t"
      "v_cvt_pk_bf16_f32 v41, v102, v103\n\t"
      "v_cvt_pk_bf16_f32 v42, v104, v105\n\t"
      "v_cvt_pk_bf16_f32 v43, v106, v107\n\t"
      "ds_write_b128 v27, v[40:43] offset:0\n\t"
      "s_nop 0\n\t"
      "v_mfma_f32_16x16x32_bf16 v[32:35], v[36:39], v[40:43], 0\n\t"
      "s_waitcnt lgkmcnt(3)\n\t"
      "v_add_f32 v108, v108, v132\n\t"
      "v_add_f32 v109, v109, v133\n\t"
      "v_add_f32 v110, v110, v134\n\t"
      "v_add_f32 v111, v111, v135\n\t"
      "v_add_f32 v112, v112, v136\n\t"
      "v_add_f32 v113, v113, v137\n\t"
      "v_add_f32 v114, v114, v138\n\t"
      "v_add_f32 v115, v115, v139\n\t"
      "v_cvt_pk_bf16_f32 v44, v108, v109\n\t"
      "v_cvt_pk_bf16_f32 v45, v110, v111\n\t"
      "v_cvt_pk_bf16_f32 v46, v112, v113\n\t"
      "v_cvt_pk_bf16_f32 v47, v114, v115\n\t"
      "ds_write_b128 v27, v[44:47] offset:1024\n\t"
      "s_nop 0\n\t"
      "v_mfma_f32_16x16x32_bf16 v[32:35], v[36:39], v[44:47], v[32:35]\n\t"
      "s_waitcnt lgkmcnt(2)\n\t"
      "v_add_f32 v116, v116, v140\n\t"
      "v_add_f32 v117, v117, v141\n\t"
      "v_add_f32 v118, v118, v142\n\t"
      "v_add_f32 v119, v119, v143\n\t"
      "v_add_f32 v120, v120, v144\n\t"
      "v_add_f32 v121, v121, v145\n\t"
      "v_add_f32 v122, v122, v146\n\t"
      "v_add_f32 v123, v123, v147\n\t"
      "v_cvt_pk_bf16_f32 v48, v116, v117\n\t"
      "v_cvt_pk_bf16_f32 v49, v118, v119\n\t"
      "v_cvt_pk_bf16_f32 v50, v120, v121\n\t"
      "v_cvt_pk_bf16_f32 v51, v122, v123\n\t"
      "ds_write_b128 v27, v[48:51] offset:2048\n\t"
      "s_nop 0\n\t"
      "v_mfma_f32_16x16x32_bf16 v[32:35], v[36:39], v[48:51], v[32:35]\n\t"
      "ds_write2st64_b32 v28, v148, v150 offset1:1\n\t"
      "s_waitcnt vmcnt(0)\n\t"
      "s_nop 5\n\t"
      "ds_write_b32 v28, v32 offset:512\n\t"
      "s_waitcnt lgkmcnt(0)\n\t"
      "s_barrier\n\t"
      "ds_read_b32 v152, v160 offset:16\n\t"
      "s_waitcnt lgkmcnt(0)\n\t"
      "v_cmp_eq_u32 vcc, 0, v152\n\t"
      "s_cbranch_vccnz END%=\n\t"
      "v_lshl_add_u32 v152, %[ma], 2, s82\n\t"
      "s_and_b32 s79, %[s], 3\n\t"
      "s_lshl_b32 s79, s79, 2\n\t"
      "v_subrev_u32 v152, s79, v152\n\t"
      "ds_read_b128 v[124:127], v152\n\t"
      "s_waitcnt lgkmcnt(0)\n\t"
      "v_max3_f32 v154, v124, v125, v126\n\t"
      "v_max_f32 v154, v154, v127\n\t"
      "v_sub_f32 v152, v148, v154\n\t"
      "v_exp_f32 v152, v152\n\t"
      "v_mov_b32 v148, v154\n\t"
      "v_mul_f32 v150, v150, v152\n\t"
      "v_pk_mul_f32 v[100:101], v[100:101], v[152:153] op_sel_hi:[1,0]\n\t"
      "v_pk_mul_f32 v[102:103], v[102:103], v[152:153] op_sel_hi:[1,0]\n\t"
      "v_pk_mul_f32 v[104:105], v[104:105], v[152:153] op_sel_hi:[1,0]\n\t"
      "v_pk_mul_f32 v[106:107], v[106:107], v[152:153] op_sel_hi:[1,0]\n\t"
      "v_pk_mul_f32 v[108:109], v[108:109], v[152:153] op_sel_hi:[1,0]\n\t"
      "v_pk_mul_f32 v[110:111], v[110:111], v[152:153] op_sel_hi:[1,0]\n\t"
      "v_pk_mul_f32 v[112:113], v[112:113], v[152:153] op_sel_hi:[1,0]\n\t"
      "v_pk_mul_f32 v[114:115], v[114:115], v[152:153] op_sel_hi:[1,0]\n\t"
      "v_pk_mul_f32 v[116:117], v[116:117], v[152:153] op_sel_hi:[1,0]\n\t"
      "v_pk_mul_f32 v[118:119], v[118:119], v[152:153] op_sel_hi:[1,0]\n\t"
      "v_pk_mul_f32 v[120:121], v[120:121], v[152:153] op_sel_hi:[1,0]\n\t"
      "v_pk_mul_f32 v[122:123], v[122:123], v[152:153] op_sel_hi:[1,0]\n\t"
      "s_barrier\n\t"
      "v_cvt_pk_bf16_f32 v40, v100, v101\n\t"
      "v_cvt_pk_bf16_f32 v41, v102, v103\n\t"
      "v_cvt_pk_bf16_f32 v42, v104, v105\n\t"
      "v_cvt_pk_bf16_f32 v43, v106, v107\n\t"
      "ds_write_b128 v27, v[40:43] offset:0\n\t"
      "s_nop 0\n\t"
      "v_mfma_f32_16x16x32_bf16 v[32:35], v[36:39], v[40:43], 0\n\t"
      "v_cvt_pk_bf16_f32 v44, v108, v109\n\t"
      "v_cvt_pk_bf16_f32 v45, v110, v111\n\t"
      "v_cvt_pk_bf16_f32 v46, v112, v113\n\t"
      "v_cvt_pk_bf16_f32 v47, v114, v115\n\t"
      "ds_write_b128 v27, v[44:47] offset:1024\n\t"
      "s_nop 0\n\t"
      "v_mfma_f32_16x16x32_bf16 v[32:35], v[36:39], v[44:47], v[32:35]\n\t"
      "v_cvt_pk_bf16_f32 v48, v116, v117\n\t"
      "v_cvt_pk_bf16_f32 v49, v118, v119\n\t"
      "v_cvt_pk_bf16_f32 v50, v120, v121\n\t"
      "v_cvt_pk_bf16_f32 v51, v122, v123\n\t"
      "ds_write_b128 v27, v[48:51] offset:2048\n\t"
      "s_nop 0\n\t"
      "v_mfma_f32_16x16x32_bf16 v[32:35], v[36:39], v[48:51], v[32:35]\n\t"
      "ds_write2st64_b32 v28, v148, v150 offset1:1\n\t"
      "s_waitcnt vmcnt(0)\n\t"
      "s_nop 5\n\t"
      "ds_write_b32 v28, v32 offset:512\n\t"
      "s_waitcnt lgkmcnt(0)\n\t"
      "s_barrier\n\t"
      "s_branch END%=\n\t"
      "ABX%=:\n\t"
      "s_waitcnt lgkmcnt(0)\n\t"
      "v_cmp_ne_u32 vcc, 0, v153\n\t"
      "s_cbranch_vccnz ABY%=\n\t"
      "ds_read_b32 v153, v26 offset:32\n\t"
      "ds_read_b64 v[154:155], v25\n\t"
      "ds_read_b128 v[124:127], v24 offset:0\n\t"
      "ds_read_b128 v[128:131], v24 offset:1024\n\t"
      "ds_read_b128 v[132:135], v24 offset:2048\n\t"
      "ds_read_b128 v[136:139], v24 offset:3072\n\t"
      "ds_read_b128 v[140:143], v24 offset:4096\n\t"
      "ds_read_b128 v[144:147], v24 offset:5120\n\t"
      "s_branch ABX%=\n\t"
      "ABY%=:\n\t"
      "v_max_f32 v156, v148, v154\n\t"
      "v_sub_f32 v152, v148, v156\n\t"
      "v_sub_f32 v154, v154, v156\n\t"
      "v_exp_f32 v152, v152\n\t"
      "v_exp_f32 v154, v154\n\t"
      "v_mov_b32 v148, v156\n\t"
      "v_mul_f32 v150, v150, v152\n\t"
      "v_mul_f32 v155, v155, v154\n\t"
      "v_pk_mul_f32 v[100:101], v[100:101], v[152:153] op_sel_hi:[1,0]\n\t"
      "v_pk_mul_f32 v[124:125], v[124:125], v[154:155] op_sel_hi:[1,0]\n\t"
      "v_pk_mul_f32 v[102:103], v[102:103], v[152:153] op_sel_hi:[1,0]\n\t"
      "v_pk_mul_f32 v[126:127], v[126:127], v[154:155] op_sel_hi:[1,0]\n\t"
      "v_pk_mul_f32 v[104:105], v[104:105], v[152:153] op_sel_hi:[1,0]\n\t"
      "v_pk_mul_f32 v[128:129], v[128:129], v[154:155] op_sel_hi:[1,0]\n\t"
      "v_pk_mul_f32 v[106:107], v[106:107], v[152:153] op_sel_hi:[1,0]\n\t"
      "v_pk_mul_f32 v[130:131], v[130:131], v[154:155] op_sel_hi:[1,0]\n\t"
      "v_pk_mul_f32 v[108:109], v[108:109], v[152:153] op_sel_hi:[1,0]\n\t"
      "v_pk_mul_f32 v[132:133], v[132:133], v[154:155] op_sel_hi:[1,0]\n\t"
      "v_pk_mul_f32 v[110:111], v[110:111], v[152:153] op_sel_hi:[1,0]\n\t"
      "v_pk_mul_f32 v[134:135], v[134:135], v[154:155] op_sel_hi:[1,0]\n\t"
      "v_pk_mul_f32 v[112:113], v[112:113], v[152:153] op_sel_hi:[1,0]\n\t"
      "v_pk_mul_f32 v[136:137], v[136:137], v[154:155] op_sel_hi:[1,0]\n\t"
      "v_pk_mul_f32 v[114:115], v[114:115], v[152:153] op_sel_hi:[1,0]\n\t"
      "v_pk_mul_f32 v[138:139], v[138:139], v[154:155] op_sel_hi:[1,0]\n\t"
      "v_pk_mul_f32 v[116:117], v[116:117], v[152:153] op_sel_hi:[1,0]\n\t"
      "v_pk_mul_f32 v[140:141], v[140:141], v[154:155] op_sel_hi:[1,0]\n\t"
      "v_pk_mul_f32 v[118:119], v[118:119], v[152:153] op_sel_hi:[1,0]\n\t"
      "v_pk_mul_f32 v[142:143], v[142:143], v[154:155] op_sel_hi:[1,0]\n\t"
      "v_pk_mul_f32 v[120:121], v[120:121], v[152:153] op_sel_hi:[1,0]\n\t"
      "v_pk_mul_f32 v[144:145], v[144:145], v[154:155] op_sel_hi:[1,0]\n\t"
      "v_pk_mul_f32 v[122:123], v[122:123], v[152:153] op_sel_hi:[1,0]\n\t"
      "v_pk_mul_f32 v[146:147], v[146:147], v[154:155] op_sel_hi:[1,0]\n\t"
      "s_branch ABK%=\n\t"
      "AEND%=:\n\t"
      "v_mov_b32 v152, v150\n\t"
      "s_nop 1\n\t"
      "v_permlane16_swap_b32 v150, v152\n\t"
      "v_add_f32 v150, v150, v152\n\t"
      "v_mov_b32 v152, v150\n\t"
      "s_nop 1\n\t"
      "v_permlane32_swap_b32 v150, v152\n\t"
      "v_add_f32 v150, v150, v152\n\t"
      "v_mov_b32 v149, v150\n\t"
      "v_mov_b32 v152, 1\n\t"
      "ds_write_b128 v24, v[100:103] offset:0\n\t"
      "ds_write_b128 v24, v[104:107] offset:1024\n\t"
      "ds_write_b128 v24, v[108:111] offset:2048\n\t"
      "ds_write_b128 v24, v[112:115] offset:3072\n\t"
      "ds_write_b128 v24, v[116:119] offset:4096\n\t"
      "ds_write_b128 v24, v[120:123] offset:5120\n\t"
      "ds_write_b64 v25, v[148:149]\n\t"
      "ds_write_b32 v26, v152 offset:32\n\t"
      "END%=:\n\t"
      : [ao] "+v"(aoff), [fo] "+v"(foff), [tE] "+v"(tE), [tO] "+v"(tO), [fE] "+v"(fE), [fO] "+v"(fO)
      : [vl] "v"(vl), [ma] "v"(ma), [s] "s"(s)
      : "memory", "vcc", "scc", "v8", "v9", "v10", "v11", "v12", "v13", "v14", "v15", "v16", "v17", "v18", "v19", "v20", "v21", "v22", "v23", "v24", "v25", "v26", "v27", "v28", "v29", "v30", "v31", "v32", "v33", "v34", "v35", "v36", "v37", "v38", "v39", "v40", "v41", "v42", "v43", "v44", "v45", "v46", "v47", "v48", "v49", "v50", "v51", "v52", "v53", "v54", "v55", "v56", "v57", "v58", "v59", "v60", "v61", "v62", "v63", "v64", "v65", "v66", "v67", "v68", "v69", "v70", "v71", "v72", "v73", "v74", "v75", "v76", "v77", "v78", "v79", "v80", "v81", "v82", "v83", "v84", "v85", "v86", "v87", "v88", "v89", "v90", "v91", "v92", "v93", "v94", "v95", "v96", "v97", "v98", "v99", "v100", "v101", "v102", "v103", "v104", "v105", "v106", "v107", "v108", "v109", "v110", "v111", "v112", "v113", "v114", "v115", "v116", "v117", "v118", "v119", "v120", "v121", "v122", "v123", "v124", "v125", "v126", "v127", "v128", "v129", "v130", "v131", "v132", "v133", "v134", "v135", "v136", "v137", "v138", "v139", "v140", "v141", "v142", "v143", "v144", "v145", "v146", "v147", "v148", "v149", "v150", "v151", "v152", "v153", "v154", "v155", "v156", "v157", "v158", "v159", "v160", "s60", "s61", "s62", "s63", "s64", "s65", "s66", "s67", "s68", "s69", "s70", "s71", "s72", "s73", "s74", "s75", "s76", "s77", "s78", "s79", "s80", "s81", "s82");
    }
    if (wave >= 8 || (wave >= 6 && b >= NCONS)) { STAMP_OUT(); return; }
  }
  asm volatile(
      "v_and_b32 v8, 63, %[tid]\n\t"
      "v_and_b32 v9, 15, v8\n\t"
      "v_lshrrev_b32 v10, 4, v8\n\t"
      "v_bfe_u32 v11, v8, 2, 2\n\t"
      "v_and_b32 v12, 3, v8\n\t"
      "v_lshlrev_b32 v13, 4, v8\n\t"
      "s_mov_b32 s60, %[wlo]\n\t"
      "s_and_b32 s61, %[whi], 0xffff\n\t"
      "s_mov_b32 s62, 1597504\n\t"
      "s_mov_b32 s63, 0x20000\n\t"
      "v_mov_b32 v21, 161856\n\t"
      "s_cmp_gt_u32 %[w], 5\n\t"
      "s_cbranch_scc1 PB%=\n\t"
      "v_lshl_add_u32 v14, v10, 2, v11\n\t"
      "v_mul_u32_u24 v14, 192, v14\n\t"
      "s_lshl_b32 s68, %[w], 5\n\t"
      "v_lshl_add_u32 v14, v12, 3, v14\n\t"
      "v_add_u32 v14, s68, v14\n\t"
      "v_add_u32 v15, 138240, v14\n\t"
      "v_lshrrev_b32 v30, 2, v9\n\t"
      "v_mul_u32_u24 v30, 24, v30\n\t"
      "s_lshl_b32 s68, %[w], 2\n\t"
      "v_add3_u32 v30, v30, v10, s68\n\t"
      "v_mul_u32_u24 v16, 16640, v30\n\t"
      "s_lshl_b32 s68, %[b], 6\n\t"
      "v_lshl_add_u32 v16, v12, 4, v16\n\t"
      "v_add_u32 v16, s68, v16\n\t"
      "v_add_u32 v17, 110592, v13\n\t"
      "v_add_u32 v18, 156672, v13\n\t"
      "s_lshr_b32 s68, %[w], 1\n\t"
      "s_lshl_b32 s68, s68, 10\n\t"
      "s_and_b32 s69, %[w], 1\n\t"
      "s_lshl_b32 s69, s69, 3\n\t"
      "s_add_u32 s68, s68, s69\n\t"
      "v_add_u32 v19, s68, v18\n\t"
      "v_mov_b32 v30, 160256\n\t"
      "v_lshl_add_u32 v20, v9, 4, v30\n\t"
      "v_mov_b32 v30, 161024\n\t"
      "v_lshl_add_u32 v28, v9, 2, v30\n\t"
      "v_mov_b32 v84, 0x3f803f80\n\t"
      "v_mov_b32 v85, 0x3f803f80\n\t"
      "v_mov_b32 v86, 0x3f803f80\n\t"
      "v_mov_b32 v87, 0x3f803f80\n\t"
      "s_waitcnt vmcnt(0)\n\t"
      "s_branch VWb%=\n\t"
      "VWa%=:\n\t"
      "s_sleep 1\n\t"
      "VWb%=:\n\t"
      "ds_read_b32 v30, v21 offset:24\n\t"
      "s_waitcnt lgkmcnt(0)\n\t"
      "v_cmp_eq_u32 vcc, 0, v30\n\t"
      "s_cbranch_vccnz VWa%=\n\t"
      "ds_read_b64_tr_b16 v[36:37], v15 offset:0\n\t"
      "ds_read_b64_tr_b16 v[38:39], v15 offset:3072\n\t"
      "ds_read_b64_tr_b16 v[40:41], v15 offset:6144\n\t"
      "ds_read_b64_tr_b16 v[42:43], v15 offset:9216\n\t"
      "ds_read_b64_tr_b16 v[44:45], v14 offset:18432\n\t"
      "ds_read_b64_tr_b16 v[46:47], v14 offset:21504\n\t"
      "ds_read_b64_tr_b16 v[48:49], v14 offset:24576\n\t"
      "ds_read_b64_tr_b16 v[50:51], v14 offset:27648\n\t"
      "ds_read_b64_tr_b16 v[52:53], v14 offset:33792\n\t"
      "ds_read_b64_tr_b16 v[54:55], v14 offset:36864\n\t"
      "ds_read_b64_tr_b16 v[56:57], v14 offset:39936\n\t"
      "ds_read_b64_tr_b16 v[58:59], v14 offset:43008\n\t"
      "PA%=:\n\t"
      "s_mov_b32 s72, 0\n\t"
      "v_mov_b32 v34, v16\n\t"
      "s_waitcnt lgkmcnt(0)\n\t"
      "s_barrier\n\t"
      "PD%=:\n\t"
      "ds_read_b64 v[76:77], v21 offset:16\n\t"
      "ds_read_b128 v[88:91], v17 offset:0\n\t"
      "ds_read_b128 v[92:95], v17 offset:1024\n\t"
      "ds_read_b128 v[96:99], v17 offset:2048\n\t"
      "ds_read_b128 v[100:103], v17 offset:6144\n\t"
      "ds_read_b128 v[104:107], v17 offset:7168\n\t"
      "ds_read_b128 v[108:111], v17 offset:8192\n\t"
      "ds_read_b128 v[112:115], v17 offset:15360\n\t"
      "ds_read_b128 v[116:119], v17 offset:16384\n\t"
      "ds_read_b128 v[120:123], v17 offset:17408\n\t"
      "ds_read_b128 v[124:127], v17 offset:21504\n\t"
      "ds_read_b128 v[128:131], v17 offset:22528\n\t"
      "ds_read_b128 v[132:135], v17 offset:23552\n\t"
      "ds_read_b128 v[68:71], v20 offset:512\n\t"
      "ds_read_b128 v[72:75], v20 offset:256\n\t"
      "ds_read_b32 v78, v20\n\t"
      "v_mfma_f32_16x16x32_bf16 v[60:63], v[36:39], v[84:87], 0\n\t"
      "v_mfma_f32_16x16x32_bf16 v[60:63], v[40:43], v[84:87], v[60:63]\n\t"
      "v_mfma_f32_16x16x32_bf16 v[60:63], v[44:47], v[84:87], v[60:63]\n\t"
      "s_waitcnt lgkmcnt(14)\n\t"
      "v_cmp_ne_u32 vcc, 0, v76\n\t"
      "v_mfma_f32_16x16x32_bf16 v[64:67], v[36:39], v[88:91], 0\n\t"
      "s_cbranch_vccnz PS%=\n\t"
      "PSB%=:\n\t"
      "s_waitcnt lgkmcnt(13)\n\t"
      "v_mfma_f32_16x16x32_bf16 v[64:67], v[40:43], v[92:95], v[64:67]\n\t"
      "s_waitcnt lgkmcnt(12)\n\t"
      "v_mfma_f32_16x16x32_bf16 v[64:67], v[44:47], v[96:99], v[64:67]\n\t"
      "s_waitcnt lgkmcnt(11)\n\t"
      "v_mfma_f32_16x16x32_bf16 v[64:67], v[36:39], v[100:103], v[64:67]\n\t"
      "s_waitcnt lgkmcnt(10)\n\t"
      "v_mfma_f32_16x16x32_bf16 v[64:67], v[40:43], v[104:107], v[64:67]\n\t"
      "s_waitcnt lgkmcnt(9)\n\t"
      "v_mfma_f32_16x16x32_bf16 v[64:67], v[44:47], v[108:111], v[64:67]\n\t"
      "s_waitcnt lgkmcnt(8)\n\t"
      "v_mfma_f32_16x16x32_bf16 v[64:67], v[36:39], v[112:115], v[64:67]\n\t"
      "s_waitcnt lgkmcnt(7)\n\t"
      "v_mfma_f32_16x16x32_bf16 v[64:67], v[40:43], v[116:119], v[64:67]\n\t"
      "s_waitcnt lgkmcnt(6)\n\t"
      "v_mfma_f32_16x16x32_bf16 v[64:67], v[44:47], v[120:123], v[64:67]\n\t"
      "s_waitcnt lgkmcnt(5)\n\t"
      "v_mfma_f32_16x16x32_bf16 v[64:67], v[36:39], v[124:127], v[64:67]\n\t"
      "s_waitcnt lgkmcnt(4)\n\t"
      "v_mfma_f32_16x16x32_bf16 v[64:67], v[40:43], v[128:131], v[64:67]\n\t"
      "s_waitcnt lgkmcnt(3)\n\t"
      "v_mfma_f32_16x16x32_bf16 v[64:67], v[44:47], v[132:135], v[64:67]\n\t"
      "s_waitcnt lgkmcnt(1)\n\t"
      "v_add_f32 v68, v68, v69\n\t"
      "v_add_f32 v70, v70, v71\n\t"
      "v_add_f32 v72, v72, v73\n\t"
      "v_add_f32 v74, v74, v75\n\t"
      "v_add_f32 v68, v68, v70\n\t"
      "v_add_f32 v72, v72, v74\n\t"
      "v_mul_f32 v68, 0xbc2aaaab, v68\n\t"
      "v_fmac_f32 v64, v60, v68\n\t"
      "v_fmac_f32 v65, v61, v68\n\t"
      "v_fmac_f32 v66, v62, v68\n\t"
      "v_fmac_f32 v67, v63, v68\n\t"
      "v_cvt_pk_bf16_f32 v30, v64, v65\n\t"
      "v_cvt_pk_bf16_f32 v31, v66, v67\n\t"
      "ds_write_b64 v19, v[30:31]\n\t"
      "v_log_f32 v82, v72\n\t"
      "v_rcp_f32 v79, v72\n\t"
      "s_waitcnt lgkmcnt(1)\n\t"
      "v_add_f32 v82, v82, v78\n\t"
      "v_mul_f32 v79, 0x411cc471, v79\n\t"
      "ds_write_b32 v28, v82\n\t"
      "v_xor_b32 v83, v82, v77\n\t"
      "s_waitcnt lgkmcnt(0)\n\t"
      "s_barrier\n\t"
      "PE%=:\n\t"
      "ds_read_b128 v[88:91], v18 offset:0\n\t"
      "ds_read_b128 v[92:95], v18 offset:1024\n\t"
      "ds_read_b128 v[96:99], v18 offset:2048\n\t"
      "s_waitcnt lgkmcnt(2)\n\t"
      "v_mfma_f32_16x16x32_bf16 v[64:67], v[48:51], v[88:91], 0\n\t"
      "s_waitcnt lgkmcnt(1)\n\t"
      "v_mfma_f32_16x16x32_bf16 v[64:67], v[52:55], v[92:95], v[64:67]\n\t"
      "s_waitcnt lgkmcnt(0)\n\t"
      "v_mfma_f32_16x16x32_bf16 v[64:67], v[56:59], v[96:99], v[64:67]\n\t"
      "s_nop 7\n\t"
      "v_mul_f32 v64, v79, v64\n\t"
      "v_mul_f32 v65, v79, v65\n\t"
      "v_mul_f32 v66, v79, v66\n\t"
      "v_mul_f32 v67, v79, v67\n\t"
      "v_cvt_pk_bf16_f32 v80, v64, v65\n\t"
      "v_cvt_pk_bf16_f32 v81, v66, v67\n\t"
      "v_bitop3_b32 v83, v80, v81, v83 bitop3:0x96\n\t"
      "buffer_store_dwordx4 v[80:83], v34, s[60:63], 0 offen sc1\n\t"
      "s_cmp_lt_u32 %[b], 96\n\t"
      "s_cbranch_scc1 CB%=\n\t"
      "s_branch END%=\n\t"
      "PS%=:\n\t"
      "s_waitcnt lgkmcnt(0)\n\t"
      "s_barrier\n\t"
      "s_barrier\n\t"
      "ds_read_b128 v[88:91], v17 offset:0\n\t"
      "ds_read_b128 v[92:95], v17 offset:1024\n\t"
      "ds_read_b128 v[96:99], v17 offset:2048\n\t"
      "ds_read_b128 v[100:103], v17 offset:6144\n\t"
      "ds_read_b128 v[104:107], v17 offset:7168\n\t"
      "ds_read_b128 v[108:111], v17 offset:8192\n\t"
      "ds_read_b128 v[112:115], v17 offset:15360\n\t"
      "ds_read_b128 v[116:119], v17 offset:16384\n\t"
      "ds_read_b128 v[120:123], v17 offset:17408\n\t"
      "ds_read_b128 v[124:127], v17 offset:21504\n\t"
      "ds_read_b128 v[128:131], v17 offset:22528\n\t"
      "ds_read_b128 v[132:135], v17 offset:23552\n\t"
      "ds_read_b128 v[68:71], v20 offset:512\n\t"
      "ds_read_b128 v[72:75], v20 offset:256\n\t"
      "ds_read_b32 v78, v20\n\t"
      "s_waitcnt lgkmcnt(0)\n\t"
      "v_mfma_f32_16x16x32_bf16 v[64:67], v[36:39], v[88:91], 0\n\t"
      "s_branch PSB%=\n\t"
      "PB%=:\n\t"
      "s_waitcnt lgkmcnt(0)\n\t"
      "s_barrier\n\t"
      "s_sleep 3\n\t"
      "CB%=:\n\t"
      "s_mov_b32 s64, %[olo]\n\t"
      "s_and_b32 s65, %[ohi], 0xffff\n\t"
      "s_movk_i32 s66, 3072\n\t"
      "s_mov_b32 s67, 0x20000\n\t"
      "s_mul_i32 s68, %[b], 2731\n\t"
      "s_lshr_b32 s68, s68, 16\n\t"
      "s_mul_i32 s69, s68, 24\n\t"
      "s_sub_u32 s69, %[b], s69\n\t"
      "s_mul_i32 s70, s68, 768\n\t"
      "s_lshl_b32 s69, s69, 3\n\t"
      "s_add_u32 s70, s70, s69\n\t"
      "s_mul_i32 s71, %[b], 16640\n\t"
      "s_lshl_b32 s69, %[w], 10\n\t"
      "s_add_u32 s71, s71, s69\n\t"
      "v_add_u32 v22, s71, v13\n\t"
      "v_lshl_add_u32 v23, v10, 2, v12\n\t"
      "s_lshl_b32 s69, %[w], 2\n\t"
      "s_add_u32 s69, s69, 161088\n\t"
      "v_lshl_add_u32 v23, v23, 5, s69\n\t"
      "v_lshl_add_u32 v55, v12, 5, s69\n\t"
      "v_add_u32 v29, 512, v55\n\t"
      "v_mov_b32 v30, 161088\n\t"
      "v_lshl_add_u32 v24, v9, 5, v30\n\t"
      "v_lshl_add_u32 v25, v12, 5, v30\n\t"
      "s_lshl_b32 s68, s68, 4\n\t"
      "s_add_u32 s68, s68, 161024\n\t"
      "v_lshl_add_u32 v26, v12, 2, s68\n\t"
      "v_and_b32 v30, 1, v11\n\t"
      "v_lshrrev_b32 v31, 1, v11\n\t"
      "v_lshl_or_b32 v30, v30, 1, v31\n\t"
      "v_mul_u32_u24 v27, 192, v12\n\t"
      "v_lshl_add_u32 v27, v30, 1, v27\n\t"
      "v_cmp_gt_u32 vcc, 16, v8\n\t"
      "v_mov_b32 v30, 0x7fffffff\n\t"
      "s_nop 0\n\t"
      "v_cndmask_b32 v27, v30, v27, vcc\n\t"
      "ds_read_b32 v52, v21 offset:20\n\t"
      "ds_read_b32 v53, v26\n\t"
      "s_mov_b32 s73, 0x100000\n\t"
      "s_movk_i32 s74, 8192\n\t"
      "s_mov_b32 s72, 0\n\t"
      "s_sleep 19\n\t"
      "buffer_load_dwordx4 v[36:39], v22, s[60:63], 0 offen sc1\n\t"
      "buffer_load_dwordx4 v[40:43], v22, s[60:63], s74 offen sc1\n\t"
      "s_waitcnt lgkmcnt(0)\n\t"
      "CL%=:\n\t"
      "s_waitcnt vmcnt(1)\n\t"
      "v_bitop3_b32 v30, v36, v37, v38 bitop3:0x96\n\t"
      "v_bitop3_b32 v30, v30, v39, v52 bitop3:0x96\n\t"
      "s_waitcnt vmcnt(0)\n\t"
      "v_bitop3_b32 v31, v40, v41, v42 bitop3:0x96\n\t"
      "v_bitop3_b32 v31, v31, v43, v52 bitop3:0x96\n\t"
      "v_or_b32 v32, v30, v31\n\t"
      "v_cmp_ne_u32 vcc, 0, v32\n\t"
      "s_cbranch_vccnz CR%=\n\t"
      "CD%=:\n\t"
      "v_sub_f32 v48, v38, v53\n\t"
      "v_sub_f32 v49, v42, v53\n\t"
      "v_lshlrev_b32 v44, 16, v36\n\t"
      "v_max_f32 v30, v48, v49\n\t"
      "v_exp_f32 v48, v48\n\t"
      "v_exp_f32 v49, v49\n\t"
      "v_and_b32 v45, 0xffff0000, v36\n\t"
      "v_lshlrev_b32 v46, 16, v37\n\t"
      "v_and_b32 v47, 0xffff0000, v37\n\t"
      "v_cmp_lt_f32 vcc, 0x42c80000, v30\n\t"
      "v_mul_f32 v44, v48, v44\n\t"
      "v_mul_f32 v45, v48, v45\n\t"
      "v_mul_f32 v46, v48, v46\n\t"
      "v_mul_f32 v47, v48, v47\n\t"
      "v_add_f32 v55, v48, v49\n\t"
      "v_lshlrev_b32 v30, 16, v40\n\t"
      "v_and_b32 v31, 0xffff0000, v40\n\t"
      "v_lshlrev_b32 v32, 16, v41\n\t"
      "v_and_b32 v33, 0xffff0000, v41\n\t"
      "v_mov_b32 v48, v55\n\t"
      "v_fmac_f32 v44, v49, v30\n\t"
      "v_fmac_f32 v45, v49, v31\n\t"
      "v_fmac_f32 v46, v49, v32\n\t"
      "v_fmac_f32 v47, v49, v33\n\t"
      "v_permlane32_swap_b32 v55, v48\n\t"
      "v_permlane32_swap_b32 v44, v45\n\t"
      "v_permlane32_swap_b32 v46, v47\n\t"
      "v_add_f32 v55, v55, v48\n\t"
      "v_add_f32 v44, v44, v45\n\t"
      "v_add_f32 v46, v46, v47\n\t"
      "v_mov_b32 v48, v55\n\t"
      "s_nop 0\n\t"
      "v_permlane16_swap_b32 v44, v46\n\t"
      "v_permlane16_swap_b32 v55, v48\n\t"
      "v_add_f32 v54, v44, v46\n\t"
      "v_add_f32 v55, v55, v48\n\t"
      "s_nop 0\n\t"
      "v_add_f32_dpp v54, v54, v54 row_ror:4 row_mask:0xf bank_mask:0xf\n\t"
      "v_add_f32_dpp v55, v55, v55 row_ror:4 row_mask:0xf bank_mask:0xf\n\t"
      "s_nop 0\n\t"
      "v_add_f32_dpp v54, v54, v54 row_ror:8 row_mask:0xf bank_mask:0xf\n\t"
      "v_add_f32_dpp v55, v55, v55 row_ror:8 row_mask:0xf bank_mask:0xf\n\t"
      "ds_write_b32 v23, v54\n\t"
      "ds_write_b32 v29, v55\n\t"
      "s_cbranch_vccnz CO%=\n\t"
      "COB%=:\n\t"
      "s_waitcnt lgkmcnt(0)\n\t"
      "s_barrier\n\t"
      "CF%=:\n\t"
      "ds_read_b32 v30, v21 offset:12\n\t"
      "ds_read_b128 v[56:59], v24\n\t"
      "ds_read_b128 v[60:63], v24 offset:16\n\t"
      "ds_read_b128 v[64:67], v25 offset:512\n\t"
      "ds_read_b128 v[68:71], v25 offset:528\n\t"
      "s_waitcnt lgkmcnt(4)\n\t"
      "v_cmp_ne_u32 vcc, 0, v30\n\t"
      "s_cbranch_vccnz CS%=\n\t"
      "CSB%=:\n\t"
      "s_waitcnt lgkmcnt(2)\n\t"
      "v_add_f32 v56, v56, v60\n\t"
      "v_add_f32 v57, v57, v61\n\t"
      "v_add_f32 v58, v58, v62\n\t"
      "v_add_f32 v59, v59, v63\n\t"
      "s_waitcnt lgkmcnt(0)\n\t"
      "v_add_f32 v64, v64, v68\n\t"
      "v_add_f32 v65, v65, v69\n\t"
      "v_add_f32 v66, v66, v70\n\t"
      "v_add_f32 v67, v67, v71\n\t"
      "v_add_f32 v64, v64, v65\n\t"
      "v_add_f32 v66, v66, v67\n\t"
      "v_add_f32 v56, v56, v57\n\t"
      "v_add_f32 v64, v64, v66\n\t"
      "v_add_f32 v58, v58, v59\n\t"
      "v_rcp_f32 v64, v64\n\t"
      "v_add_f32 v56, v56, v58\n\t"
      "v_fma_f32 v56, v56, v64, %[qr]\n\t"
      "s_or_b32 s69, %[w], s72\n\t"
      "s_cmp_lg_u32 s69, 0\n\t"
      "v_cvt_pk_bf16_f32 v56, v56, v56\n\t"
      "s_cbranch_scc1 CN%=\n\t"
      "buffer_store_short v56, v27, s[64:67], s70 offen sc1\n\t"
      "s_cmp_lg_u32 %[b], 0\n\t"
      "s_cbranch_scc1 END%=\n\t"
      "s_mov_b32 s68, 1597440\n\t"
      "buffer_store_dword v52, off, s[60:63], s68 sc1\n\t"
      "s_branch END%=\n\t"
      "CN%=:\n\t"
      "s_branch END%=\n\t"
      "CR%=:\n\t"
      "s_sub_u32 s73, s73, 1\n\t"
      "s_cbranch_scc1 CD%=\n\t"
      "v_cmp_ne_u32 vcc, 0, v30\n\t"
      "s_mov_b64 exec, vcc\n\t"
      "s_cbranch_execz CR1%=\n\t"
      "buffer_load_dwordx4 v[36:39], v22, s[60:63], 0 offen sc1\n\t"
      "CR1%=:\n\t"
      "s_mov_b64 exec, -1\n\t"
      "v_cmp_ne_u32 vcc, 0, v31\n\t"
      "s_mov_b64 exec, vcc\n\t"
      "s_cbranch_execz CR2%=\n\t"
      "buffer_load_dwordx4 v[40:43], v22, s[60:63], s74 offen sc1\n\t"
      "CR2%=:\n\t"
      "s_mov_b64 exec, -1\n\t"
      "s_waitcnt vmcnt(0)\n\t"
      "s_branch CL%=\n\t"
      "CO%=:\n\t"
      "v_mov_b32 v30, 1\n\t"
      "ds_write_b32 v21, v30 offset:12\n\t"
      "s_branch COB%=\n\t"
      "CS%=:\n\t"
      "v_max_f32 v30, v38, v42\n\t"
      "s_nop 1\n\t"
      "v_max_f32_dpp v30, v30, v30 row_ror:4 row_mask:0xf bank_mask:0xf\n\t"
      "s_nop 1\n\t"
      "v_max_f32_dpp v30, v30, v30 row_ror:8 row_mask:0xf bank_mask:0xf\n\t"
      "s_nop 1\n\t"
      "v_mov_b32 v31, v30\n\t"
      "s_nop 1\n\t"
      "v_permlane16_swap_b32 v30, v31\n\t"
      "v_max_f32 v30, v30, v31\n\t"
      "v_mov_b32 v31, v30\n\t"
      "s_nop 1\n\t"
      "v_permlane32_swap_b32 v30, v31\n\t"
      "v_max_f32 v30, v30, v31\n\t"
      "ds_write_b32 v29, v30 offset:128\n\t"
      "s_waitcnt lgkmcnt(0)\n\t"
      "s_barrier\n\t"
      "ds_read_b128 v[56:59], v25 offset:640\n\t"
      "ds_read_b128 v[60:63], v25 offset:656\n\t"
      "s_waitcnt lgkmcnt(0)\n\t"
      "v_max3_f32 v53, v56, v57, v58\n\t"
      "v_max3_f32 v53, v53, v59, v60\n\t"
      "v_max3_f32 v53, v53, v61, v62\n\t"
      "v_max_f32 v53, v53, v63\n\t"
      "v_sub_f32 v48, v38, v53\n\t"
      "v_sub_f32 v49, v42, v53\n\t"
      "v_lshlrev_b32 v44, 16, v36\n\t"
      "v_max_f32 v30, v48, v49\n\t"
      "v_exp_f32 v48, v48\n\t"
      "v_exp_f32 v49, v49\n\t"
      "v_and_b32 v45, 0xffff0000, v36\n\t"
      "v_lshlrev_b32 v46, 16, v37\n\t"
      "v_and_b32 v47, 0xffff0000, v37\n\t"
      "v_cmp_lt_f32 vcc, 0x42c80000, v30\n\t"
      "v_mul_f32 v44, v48, v44\n\t"
      "v_mul_f32 v45, v48, v45\n\t"
      "v_mul_f32 v46, v48, v46\n\t"
      "v_mul_f32 v47, v48, v47\n\t"
      "v_add_f32 v55, v48, v49\n\t"
      "v_lshlrev_b32 v30, 16, v40\n\t"
      "v_and_b32 v31, 0xffff0000, v40\n\t"
      "v_lshlrev_b32 v32, 16, v41\n\t"
      "v_and_b32 v33, 0xffff0000, v41\n\t"
      "v_mov_b32 v48, v55\n\t"
      "v_fmac_f32 v44, v49, v30\n\t"
      "v_fmac_f32 v45, v49, v31\n\t"
      "v_fmac_f32 v46, v49, v32\n\t"
      "v_fmac_f32 v47, v49, v33\n\t"
      "v_permlane32_swap_b32 v55, v48\n\t"
      "v_permlane32_swap_b32 v44, v45\n\t"
      "v_permlane32_swap_b32 v46, v47\n\t"
      "v_add_f32 v55, v55, v48\n\t"
      "v_add_f32 v44, v44, v45\n\t"
      "v_add_f32 v46, v46, v47\n\t"
      "v_mov_b32 v48, v55\n\t"
      "s_nop 0\n\t"
      "v_permlane16_swap_b32 v44, v46\n\t"
      "v_permlane16_swap_b32 v55, v48\n\t"
      "v_add_f32 v54, v44, v46\n\t"
      "v_add_f32 v55, v55, v48\n\t"
      "s_nop 0\n\t"
      "v_add_f32_dpp v54, v54, v54 row_ror:4 row_mask:0xf bank_mask:0xf\n\t"
      "v_add_f32_dpp v55, v55, v55 row_ror:4 row_mask:0xf bank_mask:0xf\n\t"
      "s_nop 0\n\t"
      "v_add_f32_dpp v54, v54, v54 row_ror:8 row_mask:0xf bank_mask:0xf\n\t"
      "v_add_f32_dpp v55, v55, v55 row_ror:8 row_mask:0xf bank_mask:0xf\n\t"
      "ds_write_b32 v23, v54\n\t"
      "ds_write_b32 v29, v55\n\t"
      "s_waitcnt lgkmcnt(0)\n\t"
      "s_barrier\n\t"
      "ds_read_b128 v[56:59], v24\n\t"
      "ds_read_b128 v[60:63], v24 offset:16\n\t"
      "ds_read_b128 v[64:67], v25 offset:512\n\t"
      "ds_read_b128 v[68:71], v25 offset:528\n\t"
      "s_waitcnt lgkmcnt(0)\n\t"
      "s_branch CSB%=\n\t"
      "END%=:\n\t"
      :
      : [tid] "v"(tid), [qr] "v"(qres << 16), [w] "s"(wave), [b] "s"(b), [wlo] "s"(kb[0]), [whi] "s"(kb[1]), [olo] "s"(ka[14]), [ohi] "s"(ka[15]), [tin] "s"((unsigned)t_in)
      : "memory", "vcc", "scc", "v8", "v9", "v10", "v11", "v12", "v13", "v14", "v15", "v16", "v17", "v18", "v19", "v20", "v21", "v22", "v23", "v24", "v25", "v26", "v27", "v28", "v29", "v30", "v31", "v32", "v33", "v34", "v35", "v36", "v37", "v38", "v39", "v40", "v41", "v42", "v43", "v44", "v45", "v46", "v47", "v48", "v49", "v50", "v51", "v52", "v53", "v54", "v55", "v56", "v57", "v58", "v59", "v60", "v61", "v62", "v63", "v64", "v65", "v66", "v67", "v68", "v69", "v70", "v71", "v72", "v73", "v74", "v75", "v76", "v77", "v78", "v79", "v80", "v81", "v82", "v83", "v84", "v85", "v86", "v87", "v88", "v89", "v90", "v91", "v92", "v93", "v94", "v95", "v96", "v97", "v98", "v99", "v100", "v101", "v102", "v103", "v104", "v105", "v106", "v107", "v108", "v109", "v110", "v111", "v112", "v113", "v114", "v115", "v116", "v117", "v118", "v119", "v120", "v121", "v122", "v123", "v124", "v125", "v126", "v127", "v128", "v129", "v130", "v131", "v132", "v133", "v134", "v135", "s60", "s61", "s62", "s63", "s64", "s65", "s66", "s67", "s68", "s69", "s70", "s71", "s72", "s73", "s74", "s75");
  STAMP_OUT();
}

extern "C" void kernel_launch(void* const* d_in, const int* in_sizes, int n_in, void* d_out, int out_size, void* d_ws, size_t ws_size,
                              hipStream_t stream) {
  k_main<<<NWG, 640, 0, stream>>>((const u16*)d_in[1], (const u16*)d_in[2], (const u16*)d_in[0], (const u16*)d_in[3], (const u16*)d_in[4],
                                              (const u16*)d_in[5], (const u16*)d_in[6], (u16*)d_out, (char*)d_ws);
}
```
